```python
import torch

def _softmax(x, axis=-1):
    return torch.softmax(x, dim=axis)
def _gelu(x):
    return torch.nn.functional.gelu(x, approximate="tanh")
def _silu(x):
    return torch.nn.functional.silu(x)
def _sigmoid(x):
    return torch.sigmoid(x)
def _layernorm(x, axis=-1, eps=1e-5):
    m = x.mean(dim=axis, keepdim=True)
    v = x.var(dim=axis, keepdim=True, unbiased=False)
    return (x - m) / torch.sqrt(v + eps)
def _scatter_add(tgt, idx, src, axis=0):
    return tgt.clone().index_add_(axis, idx, src)
def _segment_sum(src, seg, n):
    out = torch.zeros((n,) + src.shape[1:], dtype=src.dtype)
    return out.index_add_(0, seg, src)
def _pad(x, pw):
    flat = tuple(v for pair in reversed(pw) for v in pair)
    return torch.nn.functional.pad(x, flat)

kernel_name = 'sequential_preLN__linear_attn_block__glu_gated_glu__prompt'

def setup_inputs():

    _w = (lambda a: (lambda t: t.to(torch.bfloat16) if t.is_floating_point() else t)(torch.as_tensor(a)))
    def _A_setup():
        B, S, D = 1, 2048, 576
        return {
            "x": _w((lambda *s: torch.randn(*s, dtype=torch.bfloat16) * 0.05)(B, S, D)),
            "wqkv": _w((lambda *s: torch.randn(*s, dtype=torch.bfloat16) * 0.05)(D, 3 * D)),
            "wo": _w((lambda *s: torch.randn(*s, dtype=torch.bfloat16) * 0.05)(D, D)),
        }
    _A_in = _A_setup()

    def _B_setup():
        B, S, D, F = 1, 2048, 576, 4608
        return {
            "x": _w((lambda *s: torch.randn(*s, dtype=torch.bfloat16) * 0.05)(B, S, D)),
            "w_gate": _w((lambda *s: torch.randn(*s, dtype=torch.bfloat16) * 0.05)(D, F)),
            "w_up": _w((lambda *s: torch.randn(*s, dtype=torch.bfloat16) * 0.05)(D, F)),
            "w_down": _w((lambda *s: torch.randn(*s, dtype=torch.bfloat16) * 0.05)(F, D)),
        }
    _B_in = _B_setup()

    return {
        "A_x": _A_in["x"],
        "A_wqkv": _A_in["wqkv"],
        "A_wo": _A_in["wo"],
        "B_x": _B_in["x"],
        "B_w_gate": _B_in["w_gate"],
        "B_w_up": _B_in["w_up"],
        "B_w_down": _B_in["w_down"],
    }

def reference(A_x, A_wqkv, A_wo, B_x, B_w_gate, B_w_up, B_w_down):
    def _A_ref(x, wqkv, wo):
        B, S, D = x.shape
        H = 32; Dh = D // H
        qkv = (lambda x, *d: x.permute(*d))((x @ wqkv).reshape(B, S, 3, H, Dh), 2, 0, 3, 1, 4)
        q, k, v = qkv[0], qkv[1], qkv[2]
        phi = lambda t: torch.where(t > 0, t + 1, torch.exp(t)) + 1e-6
        pq, pk = phi(q), phi(k)
        kv = torch.einsum("bhsd,bhse->bhde", pk, v)
        z = torch.einsum("bhsd->bhd", pk)
        num = torch.einsum("bhqd,bhde->bhqe", pq, kv)
        den = torch.einsum("bhqd,bhd->bhq", pq, z)[..., None] + 1e-6
        o = num / den
        return (lambda x, *d: x.permute(*d))(o, 0, 2, 1, 3).reshape(B, S, D) @ wo

    def _B_ref(x, w_gate, w_up, w_down):
        g = torch.einsum("bsd,df->bsf", x, w_gate)
        u = torch.einsum("bsd,df->bsf", x, w_up)
        h = _sigmoid(g) * u
        return torch.einsum("bsf,fd->bsd", h, w_down)

    x = A_x
    h = x + _A_ref(_layernorm(x, axis=-1), A_wqkv, A_wo)
    h2 = h + _B_ref(_layernorm(h, axis=-1), B_w_gate, B_w_up, B_w_down)
    return h2
```

```cpp
#include <hip/hip_runtime.h>
#include <hip/hip_bf16.h>
#include <stdint.h>

typedef unsigned short u16;
typedef short bf16x8 __attribute__((ext_vector_type(8)));
typedef short s16x4 __attribute__((ext_vector_type(4)));
typedef float f32x4 __attribute__((ext_vector_type(4)));
typedef float f32x2 __attribute__((ext_vector_type(2)));
typedef unsigned u32x4 __attribute__((ext_vector_type(4)));
typedef unsigned u32x2 __attribute__((ext_vector_type(2)));
typedef __bf16 bf16x2_t __attribute__((ext_vector_type(2)));
#define LAS __attribute__((address_space(3)))
#define DI __device__ __forceinline__
typedef LAS char* lptr;

#define MFMA16(a, b, c) __builtin_amdgcn_mfma_f32_16x16x32_bf16((a), (b), (c), 0, 0, 0)
#define SBAR() __builtin_amdgcn_sched_barrier(0)

constexpr int S_ = 2048, D_ = 576, F_ = 4608;

constexpr size_t WS_PQG = 0;
constexpr size_t WS_KVP = WS_PQG + 16 * 9 * 2048 * 8;
constexpr size_t WS_MEAN1 = WS_KVP + 16 * 32 * 352 * 4;
constexpr size_t WS_AF = WS_MEAN1 + 2048 * 4;
constexpr size_t WS_WEF = WS_AF + 2048 * 576 * 2;
constexpr size_t WS_PM = WS_WEF + 8 * 18 * 4608;
constexpr size_t WS_HCF = WS_PM + 32 * 2048 * 4;
constexpr size_t WS_SSQ = WS_HCF + 2048 * 576 * 2;
constexpr size_t WS_MEAN2 = WS_SSQ + 16 * 2048 * 4;
constexpr size_t WS_HHF = WS_MEAN2 + 2048 * 4;
constexpr size_t WS_PART = WS_HHF + (size_t)2048 * 4608 * 2;
constexpr size_t WS_END = WS_PART + (size_t)4 * 2048 * 576 * 4;

DI unsigned pk2(float a, float b) {
  bf16x2_t v = {(__bf16)a, (__bf16)b};
  return __builtin_bit_cast(unsigned, v);
}
DI float bflo(unsigned w) { return __uint_as_float(w << 16); }
DI float bfhi(unsigned w) { return __uint_as_float(w & 0xffff0000u); }
DI s16x4 trd(lptr p) { return __builtin_bit_cast(s16x4, __builtin_amdgcn_ds_read_tr16_b64_v4i16((LAS s16x4*)p)); }
DI bf16x8 cat8(s16x4 lo, s16x4 hi) { return __builtin_shufflevector(lo, hi, 0, 1, 2, 3, 4, 5, 6, 7); }
DI bf16x8 ldfrag(lptr p) { return *(LAS bf16x8*)p; }
DI void glds(const void* sbase, unsigned voff, unsigned lds_dst) {
  asm volatile("s_mov_b32 m0, %2\n\ts_nop 0\n\tglobal_load_lds_dwordx4 %0, %1" ::"v"(voff), "s"(sbase), "s"(lds_dst) : "memory");
}
#define WAIT_BAR(N) asm volatile("s_waitcnt vmcnt(" #N ") lgkmcnt(0)\n\ts_barrier" ::: "memory")
template <int N> DI void wait_bar() {
  asm volatile("s_waitcnt vmcnt(%0) lgkmcnt(0)\n\ts_barrier" ::"i"(N) : "memory");
}
DI void lds_bar() { asm volatile("s_waitcnt lgkmcnt(0)\n\ts_barrier" ::: "memory"); }
DI float phi_f(float t) { return (t > 0.f ? t + 1.f : __builtin_amdgcn_exp2f(t * 1.4426950408889634f)) + 1e-6f; }

constexpr int K1_PITCH = 240;
constexpr int K1_W = 576 * K1_PITCH;
constexpr int K1_ONES = K1_W;
constexpr int K1_STAT = K1_ONES + 16;
constexpr int K1_CS = K1_STAT + 1024;
constexpr int K1_LDS = K1_CS + 512;

template <int KB> DI void k1_issue(const char* xa0, const char* xa1, bf16x8 (&a)[18][2], const char* wq, const unsigned (&voff)[4],
                                   unsigned ldsw, int w) {
  asm volatile("global_load_dwordx4 %0, %1, off offset:%2" : "=v"(a[2 * KB][0]) : "v"(xa0), "i"(KB * 128) : "memory");
  asm volatile("global_load_dwordx4 %0, %1, off offset:%2" : "=v"(a[2 * KB][1]) : "v"(xa1), "i"(KB * 128) : "memory");
  asm volatile("global_load_dwordx4 %0, %1, off offset:%2" : "=v"(a[2 * KB + 1][0]) : "v"(xa0), "i"(KB * 128 + 64) : "memory");
  asm volatile("global_load_dwordx4 %0, %1, off offset:%2" : "=v"(a[2 * KB + 1][1]) : "v"(xa1), "i"(KB * 128 + 64) : "memory");
  const char* src = wq + (size_t)KB * 64 * 3456;
  const unsigned dst = ldsw + KB * 64 * K1_PITCH;
  glds(src, voff[0], dst);
  glds(src, voff[1], dst + 4096);
  glds(src, voff[2], dst + 8192);
  if (w < 3) glds(src, voff[3], dst + 12288);
}
template <int N, int KB> DI void k1_wait(bf16x8 (&a)[18][2]) {
  asm volatile("s_waitcnt vmcnt(%4) lgkmcnt(0)\n\ts_barrier"
               : "+v"(a[2 * KB][0]), "+v"(a[2 * KB][1]), "+v"(a[2 * KB + 1][0]), "+v"(a[2 * KB + 1][1])
               : "i"(N)
               : "memory");
}

__global__ __launch_bounds__(256, 1) void k1_qkv(const u16* __restrict__ x, const u16* __restrict__ wqkv, char* __restrict__ ws) {
  __shared__ __attribute__((aligned(16))) char smem[K1_LDS];
  const lptr L = (lptr)smem;
  const unsigned lds0 = (unsigned)(uintptr_t)L;
  const int tid = threadIdx.x, lane = tid & 63, w = __builtin_amdgcn_readfirstlane(tid >> 6);
  const int fr = lane & 15, fq = lane >> 4;
  const int xcd = blockIdx.x & 7, r = blockIdx.x >> 3;
  const int tt = (xcd & 3) * 4 + (r & 3), p = (xcd >> 2) * 8 + (r >> 2);
  const int o8 = (p & 1) * 8;
  const int tok0 = tt * 128 + w * 32;

  unsigned voff[4];
#pragma unroll
  for (int i = 0; i < 4; ++i) {
    int pi = w + 4 * i;
    if (pi > 14) pi = 14;
    const int c = pi * 64 + lane, row = c / 15, cc = c % 15, seg = cc / 5, ch = cc % 5;
    const int b = row >> 5, rho = row & 31, t = rho >> 4, rr = rho & 15;
    const int g = 2 * (rr & 1) + (rr >> 3), q = (rr >> 1) & 3;
    const int k = 32 * b + 8 * g + 4 * t + q;
    voff[i] = (unsigned)(k * 3456 + seg * 1152 + 72 * p - o8 + ch * 16);
  }
  const unsigned ldsw = lds0 + w * 1024;
  const char* xa0 = (const char*)x + (size_t)(tok0 + fr) * 1152 + fq * 16;
  const char* xa1 = xa0 + 16 * 1152;
  bf16x8 a[18][2];
  k1_issue<0>(xa0, xa1, a, (const char*)wqkv, voff, ldsw, w);
  k1_issue<1>(xa0, xa1, a, (const char*)wqkv, voff, ldsw, w);
  k1_issue<2>(xa0, xa1, a, (const char*)wqkv, voff, ldsw, w);
  if (tid == 0) *(LAS u32x4*)(L + K1_ONES) = (u32x4){0x3F803F80u, 0x3F803F80u, 0x3F803F80u, 0x3F803F80u};

  const int q4 = (lane & 15) >> 2, pp = lane & 3;
  const int rowb = (2 * (4 * (fq & 1) + q4) + (fq >> 1)) * K1_PITCH;
  const int bq = rowb + o8 + 8 * pp;
  int bx = pp < 3 ? rowb + 80 * pp + o8 + 64 : K1_ONES;
  const int bxs = pp < 3 ? 32 * K1_PITCH : 0, bxt = pp < 3 ? 16 * K1_PITCH : 0;
  const int sA = w, sB = (w + 4 < 7) ? w + 4 : 3;
  const int bcA = rowb + o8 + (sA >> 1) * 80 + 32 * (sA & 1) + 8 * pp;
  const int bcB = (sB == 6) ? rowb + 80 * (pp < 3 ? pp : 0) + o8 + 64 : rowb + o8 + (sB >> 1) * 80 + 32 * (sB & 1) + 8 * pp;

  f32x4 acc[7][2], gram[2], cs[2];
#pragma unroll
  for (int i = 0; i < 7; ++i) acc[i][0] = acc[i][1] = (f32x4){0.f, 0.f, 0.f, 0.f};
  gram[0] = gram[1] = cs[0] = cs[1] = (f32x4){0.f, 0.f, 0.f, 0.f};
  const bf16x8 ones = {0x3F80, 0x3F80, 0x3F80, 0x3F80, 0x3F80, 0x3F80, 0x3F80, 0x3F80};

  bf16x8 B[2][9];
  auto readB = [&](int s, bf16x8(&b)[9]) {
    const int so = s * 32 * K1_PITCH;
#pragma unroll
    for (int i = 0; i < 6; ++i) {
      const int off = so + (i >> 1) * 80 + 32 * (i & 1);
      b[i] = cat8(trd(L + bq + off), trd(L + bq + off + 16 * K1_PITCH));
    }
    b[6] = cat8(trd(L + bx), trd(L + bx + bxt));
    bx += bxs;
    b[7] = cat8(trd(L + bcA + so), trd(L + bcA + so + 16 * K1_PITCH));
    b[8] = cat8(trd(L + bcB + so), trd(L + bcB + so + 16 * K1_PITCH));
  };
  auto mma = [&](const bf16x8(&av)[2], const bf16x8(&b)[9]) {
#pragma unroll
    for (int m = 0; m < 2; ++m) {
      acc[0][m] = MFMA16(b[0], av[m], acc[0][m]);
      acc[1][m] = MFMA16(b[1], av[m], acc[1][m]);
#pragma unroll
      for (int i = 2; i < 7; ++i) acc[i][m] = MFMA16(av[m], b[i], acc[i][m]);
      gram[m] = MFMA16(av[m], av[m], gram[m]);
    }
    cs[0] = MFMA16(ones, b[7], cs[0]);
    cs[1] = MFMA16(ones, b[8], cs[1]);
  };

  k1_wait<14, 0>(a);
  readB(0, B[0]);
#define K1_PAIR(KB, NW)                                                                        \
  readB(2 * KB + 1, B[1]);                                                                     \
  mma(a[2 * KB], B[0]);                                                                        \
  if (KB < 8) {                                                                                \
    if (KB + 3 <= 8) k1_issue<(KB + 3 <= 8 ? KB + 3 : 8)>(xa0, xa1, a, (const char*)wqkv, voff, ldsw, w); \
    k1_wait<NW, (KB < 8 ? KB + 1 : 8)>(a);                                                     \
    readB(2 * KB + 2, B[0]);                                                                   \
  }                                                                                            \
  mma(a[2 * KB + 1], B[1]);
  K1_PAIR(0, 14) K1_PAIR(1, 14) K1_PAIR(2, 14) K1_PAIR(3, 14) K1_PAIR(4, 14) K1_PAIR(5, 14) K1_PAIR(6, 7) K1_PAIR(7, 0) K1_PAIR(8, 0)
#undef K1_PAIR

  LAS float* st = (LAS float*)(L + K1_STAT + w * 256);
#pragma unroll
  for (int m = 0; m < 2; ++m) {
    if ((fr >> 2) == fq) {
      const int rr = fr & 3;
      const float g = rr == 0 ? gram[m][0] : rr == 1 ? gram[m][1] : rr == 2 ? gram[m][2] : gram[m][3];
      st[(16 * m + fr) * 2 + 1] = g;
    }
    if (fr == 12) {
#pragma unroll
      for (int rr = 0; rr < 4; ++rr) st[(16 * m + 4 * fq + rr) * 2] = acc[6][m][rr];
    }
  }
  LAS float* csl = (LAS float*)(L + K1_CS);
  if (lane < 16) {
    csl[sA * 16 + lane] = cs[0][0];
    csl[(w + 4) * 16 + lane] = cs[1][0];
  }
  lds_bar();
  float meanT[2], rstdT[2];
  float meanN[2][4], rstdN[2][4];
#pragma unroll
  for (int m = 0; m < 2; ++m) {
    {
      const f32x2 v = *(LAS f32x2*)(st + (16 * m + fr) * 2);
      const float mu = v[0] * (1.f / 576.f);
      const float var = fmaxf(v[1] * (1.f / 576.f) - mu * mu, 0.f);
      meanT[m] = mu;
      rstdT[m] = __builtin_amdgcn_rsqf(var + 1e-5f);
    }
#pragma unroll
    for (int rr = 0; rr < 4; ++rr) {
      const f32x2 v = *(LAS f32x2*)(st + (16 * m + 4 * fq + rr) * 2);
      const float mu = v[0] * (1.f / 576.f);
      const float var = fmaxf(v[1] * (1.f / 576.f) - mu * mu, 0.f);
      meanN[m][rr] = mu;
      rstdN[m][rr] = __builtin_amdgcn_rsqf(var + 1e-5f);
    }
  }
  if (p == 0 && fq == 0) {
    float* mean1 = (float*)(ws + WS_MEAN1);
    mean1[tok0 + fr] = meanT[0];
    mean1[tok0 + 16 + fr] = meanT[1];
  }
  char* pqg = ws + WS_PQG + (size_t)p * 9 * 2048 * 8;
#pragma unroll
  for (int i = 0; i < 2; ++i) {
    const f32x4 c4 = *(LAS f32x4*)(csl + i * 16 + 4 * fq);
#pragma unroll
    for (int m = 0; m < 2; ++m) {
      float v[4];
#pragma unroll
      for (int rr = 0; rr < 4; ++rr) v[rr] = phi_f((acc[i][m][rr] - meanT[m] * c4[rr]) * rstdT[m]);
      u32x2 o = {pk2(v[0], v[1]), pk2(v[2], v[3])};
      *(u32x2*)(pqg + ((size_t)(4 * i + fq) * 2048 + tok0 + 16 * m + fr) * 8) = o;
    }
  }
  bf16x8 fragv[5];
#pragma unroll
  for (int i = 2; i < 7; ++i) {
    const float c1 = csl[i * 16 + fr];
    float v[2][4];
#pragma unroll
    for (int m = 0; m < 2; ++m)
#pragma unroll
      for (int rr = 0; rr < 4; ++rr) {
        float t = (acc[i][m][rr] - meanN[m][rr] * c1) * rstdN[m][rr];
        if (i < 4) t = phi_f(t);
        if (i == 6) t = fr < 8 ? phi_f(t) : (fr < 12 ? t : 1.0f);
        v[m][rr] = t;
      }
    u32x4 pkd = {pk2(v[0][0], v[0][1]), pk2(v[0][2], v[0][3]), pk2(v[1][0], v[1][1]), pk2(v[1][2], v[1][3])};
    fragv[i - 2] = __builtin_bit_cast(bf16x8, pkd);
    if (i == 6 && fr < 4) {
      u16* dst = (u16*)(pqg + ((size_t)8 * 2048 + tok0 + 4 * fq) * 8) + fr;
#pragma unroll
      for (int m = 0; m < 2; ++m) {
        dst[(16 * m + 0) * 4] = (u16)(pkd[2 * m] & 0xffff);
        dst[(16 * m + 1) * 4] = (u16)(pkd[2 * m] >> 16);
        dst[(16 * m + 2) * 4] = (u16)(pkd[2 * m + 1] & 0xffff);
        dst[(16 * m + 3) * 4] = (u16)(pkd[2 * m + 1] >> 16);
      }
    }
  }
  const f32x4 z4 = {0.f, 0.f, 0.f, 0.f};
  f32x4 T[8];
  T[0] = MFMA16(fragv[0], fragv[2], z4);
  T[1] = MFMA16(fragv[0], fragv[3], z4);
  T[2] = MFMA16(fragv[1], fragv[2], z4);
  T[3] = MFMA16(fragv[1], fragv[3], z4);
  T[4] = MFMA16(fragv[0], fragv[4], z4);
  T[5] = MFMA16(fragv[1], fragv[4], z4);
  T[6] = MFMA16(fragv[4], fragv[3], z4);
  T[7] = MFMA16(fragv[4], fragv[4], z4);
#pragma unroll
  for (int i = 0; i < 8; ++i) *(LAS f32x4*)(L + (w * 8 + i) * 1024 + lane * 16) = T[i];
  lds_bar();
  float* kvp = (float*)(ws + WS_KVP) + (size_t)(tt * 32 + 2 * p) * 352;
  for (int idx = tid; idx < 2 * 342; idx += 256) {
    const int a_ = idx >= 342, j = idx - a_ * 342;
    int d, e;
    if (j < 324) { d = j / 18; e = j - d * 18 + 18 * a_; d += 18 * a_; }
    else { d = j - 324 + 18 * a_; e = 36; }
    const int dt = d >> 4, et = e >> 4;
    const int dr = dt == 2 ? d - 32 + 4 : d & 15;
    const int ec = et == 2 ? e - 32 + 8 : e & 15;
    const int tile = et == 2 ? (dt == 2 ? 7 : 4 + dt) : (dt == 2 ? 6 : dt * 2 + et);
    const int off = tile * 1024 + (ec + 16 * (dr >> 2)) * 16 + (dr & 3) * 4;
    const float s = (*(LAS float*)(L + off) + *(LAS float*)(L + off + 8192)) + (*(LAS float*)(L + off + 16384) + *(LAS float*)(L + off + 24576));
    kvp[a_ * 352 + j] = s;
  }
}

__global__ __launch_bounds__(256) void k1b_fold(const u16* __restrict__ wo, char* __restrict__ ws) {
  __shared__ __attribute__((aligned(16))) float red[2][352];
  __shared__ __attribute__((aligned(16))) float kvz[352];
  __shared__ __attribute__((aligned(16))) float rsum[32];
  __shared__ __attribute__((aligned(16))) float rm[32];
  const int tid = threadIdx.x;
  const int h = blockIdx.x >> 3, j = blockIdx.x & 7;
  const int p = h >> 1, a = h & 1;
  const int tok = 256 * j + tid;
  u32x2 pq[5];
  const char* pqg = ws + WS_PQG + ((size_t)(p * 9 + 4 * a) * 2048 + tok) * 8;
#pragma unroll
  for (int g = 0; g < 5; ++g) pq[g] = *(const u32x2*)(pqg + (size_t)g * 2048 * 8);
  f32x4 s4 = {0.f, 0.f, 0.f, 0.f};
  const int c88 = tid % 88, hf = tid / 88;
  if (tid < 176) {
    const float* src = (const float*)(ws + WS_KVP) + (size_t)(hf * 8 * 32 + h) * 352 + c88 * 4;
    f32x4 v[8];
#pragma unroll
    for (int t = 0; t < 8; ++t) v[t] = *(const f32x4*)(src + (size_t)t * 32 * 352);
    s4 = ((v[0] + v[1]) + (v[2] + v[3])) + ((v[4] + v[5]) + (v[6] + v[7]));
  }
  float rs = 0.f;
  if (tid < 144) {
    const int e = tid >> 3, l8 = tid & 7;
    const char* src = (const char*)wo + (size_t)(18 * h + e) * 1152 + l8 * 16;
    u32x4 v[9];
#pragma unroll
    for (int i = 0; i < 9; ++i) v[i] = *(const u32x4*)(src + i * 128);
#pragma unroll
    for (int i = 0; i < 9; ++i)
#pragma unroll
      for (int q = 0; q < 4; ++q) rs += bflo(v[i][q]) + bfhi(v[i][q]);
  }
  rs += __shfl_xor(rs, 1);
  rs += __shfl_xor(rs, 2);
  rs += __shfl_xor(rs, 4);
  const int c72 = tid % 72, part = tid / 72;
  float wcol[18];
  if (tid < 216) {
#pragma unroll
    for (int e = 0; e < 18; ++e) wcol[e] = __uint_as_float((unsigned)wo[(size_t)(18 * h + e) * 576 + 72 * j + c72] << 16);
  }
  if (tid < 176) *(f32x4*)&red[hf][c88 * 4] = s4;
  if (tid < 144 && (tid & 7) == 0) rsum[tid >> 3] = rs;
  __syncthreads();
  for (int i = tid; i < 352; i += 256) kvz[i] = red[0][i] + red[1][i];
  __syncthreads();
  if (tid < 18) {
    float s = 0.f;
#pragma unroll
    for (int e = 0; e < 18; ++e) s += kvz[tid * 18 + e] * rsum[e];
    rm[tid] = s * (1.f / 576.f);
  }
  if (tid < 216) {
    const int sub = c72 >> 4, frc = c72 & 15;
    char* base = ws + WS_WEF + (size_t)j * 18 * 4608;
    if (part < 2) {
      float o[8];
#pragma unroll
      for (int i = 0; i < 8; ++i) {
        float s = 0.f;
#pragma unroll
        for (int e = 0; e < 18; ++e) s += kvz[(part * 8 + i) * 18 + e] * wcol[e];
        o[i] = s;
      }
      const int ci = 2 * h + part, ks = ci >> 2, fq = ci & 3;
      const int off = ks * 4608 + (sub < 4 ? sub * 1024 + (frc + 16 * fq) * 16 : 4096 + (frc + 8 * fq) * 16);
      *(u32x4*)(base + off) = (u32x4){pk2(o[0], o[1]), pk2(o[2], o[3]), pk2(o[4], o[5]), pk2(o[6], o[7])};
    } else {
      float o[2];
#pragma unroll
      for (int i = 0; i < 2; ++i) {
        float s = 0.f;
#pragma unroll
        for (int e = 0; e < 18; ++e) s += kvz[(16 + i) * 18 + e] * wcol[e];
        o[i] = s;
      }
      const int ci = 64 + (h >> 2), ks = ci >> 2, fq = ci & 3;
      const int off = ks * 4608 + (sub < 4 ? sub * 1024 + (frc + 16 * fq) * 16 : 4096 + (frc + 8 * fq) * 16) + (h & 3) * 4;
      *(unsigned*)(base + off) = pk2(o[0], o[1]);
    }
  }
  __syncthreads();
  const unsigned am = a ? 0xffffffffu : 0u;
  float qq[18];
#pragma unroll
  for (int i = 0; i < 9; ++i) {
    const unsigned w0 = pq[i >> 1][i & 1], w1 = pq[(i + 1) >> 1][(i + 1) & 1];
    const unsigned wv = (w1 & am) | (w0 & ~am);
    qq[2 * i] = bflo(wv);
    qq[2 * i + 1] = bfhi(wv);
  }
  float den = 0.f;
#pragma unroll
  for (int d = 0; d < 18; ++d) den += qq[d] * kvz[324 + d];
  den += 1e-6f;
  const float inv = 1.0f / den;
  unsigned pk[9];
  float pmv = 0.f;
#pragma unroll
  for (int d = 0; d < 9; ++d) {
    pk[d] = pk2(qq[2 * d] * inv, qq[2 * d + 1] * inv);
    pmv += bflo(pk[d]) * rm[2 * d] + bfhi(pk[d]) * rm[2 * d + 1];
  }
  ((float*)(ws + WS_PM))[(size_t)h * 2048 + tok] = pmv;
  const int t64 = tok >> 6, msub = (tok >> 4) & 3, fr = tok & 15;
  char* af = ws + WS_AF + (size_t)t64 * 18 * 4096 + msub * 1024;
  {
    const int ks = h >> 1, fq = 2 * (h & 1);
    char* d0 = af + ks * 4096 + (fr + 16 * fq) * 16;
    *(u32x4*)d0 = (u32x4){pk[0], pk[1], pk[2], pk[3]};
    *(u32x4*)(d0 + 256) = (u32x4){pk[4], pk[5], pk[6], pk[7]};
    const int ci = 64 + (h >> 2);
    *(unsigned*)(af + (ci >> 2) * 4096 + (fr + 16 * (ci & 3)) * 16 + (h & 3) * 4) = pk[8];
  }
}

constexpr int K2_STEP = 4096 + 4608;
constexpr int K2_LDS = 18 * K2_STEP;

__global__ __launch_bounds__(512, 2) void k2_wo(const u16* __restrict__ x, char* __restrict__ ws) {
  __shared__ __attribute__((aligned(16))) char smem[K2_LDS];
  const lptr L = (lptr)smem;
  const unsigned lds0 = (unsigned)(uintptr_t)L;
  const int tid = threadIdx.x, lane = tid & 63, w = __builtin_amdgcn_readfirstlane(tid >> 6);
  const int fr = lane & 15, fq = lane >> 4;
  const int xcd = blockIdx.x & 7, r = blockIdx.x >> 3;
  const int t64 = (xcd & 3) * 8 + (r & 7), j = (xcd >> 2) * 4 + (r >> 3);
  const int msub = w & 3, kh = w >> 2;
  const int tok = t64 * 64 + msub * 16 + fr;

  const char* srcA = ws + WS_AF + (size_t)t64 * 18 * 4096;
  const char* srcB = ws + WS_WEF + (size_t)j * 18 * 4608;
  const unsigned vl = lane * 16;
#pragma unroll
  for (int kb = 0; kb < 9; ++kb) {
    glds(srcA + kb * 8192 + w * 1024, vl, lds0 + kb * 8192 + w * 1024);
    glds(srcB + kb * 9216 + w * 1024, vl, lds0 + 18 * 4096 + kb * 9216 + w * 1024);
    if (w == (kb & 7)) glds(srcB + kb * 9216 + 8192, vl, lds0 + 18 * 4096 + kb * 9216 + 8192);
  }
  float pmv[8];
  const float* pm = (const float*)(ws + WS_PM) + (size_t)(8 * fq) * 2048 + tok;
#pragma unroll
  for (int i = 0; i < 8; ++i) pmv[i] = pm[(size_t)i * 2048];
  const float mean1 = ((const float*)(ws + WS_MEAN1))[tok];
  u32x2 xv[3];
  const char* xr = (const char*)x + (size_t)tok * 1152 + 144 * j + 8 * fq;
  xv[0] = *(const u32x2*)(xr + kh * 64);
  xv[1] = *(const u32x2*)(xr + kh * 64 + 32);
  xv[2] = *(const u32x2*)((const char*)x + (size_t)tok * 1152 + 144 * j + 128 + 8 * (fq & 1));
  SBAR();

  f32x4 acc[5];
#pragma unroll
  for (int i = 0; i < 5; ++i) acc[i] = (f32x4){0.f, 0.f, 0.f, 0.f};
  const int aoff = kh * 4096 + msub * 1024 + lane * 16;
  const int boff = 18 * 4096 + kh * 4608 + lane * 16;
  const int b4off = 18 * 4096 + kh * 4608 + 4096 + ((fr & 7) + 8 * fq) * 16;
#define K2_STEPM(KB)                                                         \
  {                                                                          \
    wait_bar<2 * (8 - KB) + 12>();                                           \
    const bf16x8 av = ldfrag(L + aoff + KB * 8192);                          \
    _Pragma("unroll") for (int i = 0; i < 4; ++i) {                          \
      const bf16x8 bv = ldfrag(L + boff + KB * 9216 + i * 1024);             \
      acc[i] = MFMA16(bv, av, acc[i]);                                       \
    }                                                                        \
    const bf16x8 bv = ldfrag(L + b4off + KB * 9216);                         \
    acc[4] = MFMA16(bv, av, acc[4]);                                         \
  }
  K2_STEPM(0) K2_STEPM(1) K2_STEPM(2) K2_STEPM(3) K2_STEPM(4) K2_STEPM(5) K2_STEPM(6) K2_STEPM(7) K2_STEPM(8)
#undef K2_STEPM
  SBAR();
  lds_bar();
  {
    const lptr xs = L + (msub * 2 + kh) * 3072 + lane * 16;
    if (kh == 0) {
      *(LAS f32x4*)(xs) = acc[2];
      *(LAS f32x4*)(xs + 1024) = acc[3];
    } else {
      *(LAS f32x4*)(xs) = acc[0];
      *(LAS f32x4*)(xs + 1024) = acc[1];
      *(LAS f32x4*)(xs + 2048) = acc[4];
    }
  }
  lds_bar();
  f32x4 o[3];
  {
    const lptr xs = L + (msub * 2 + (1 - kh)) * 3072 + lane * 16;
    o[0] = (kh == 0 ? acc[0] : acc[2]) + *(LAS f32x4*)(xs);
    o[1] = (kh == 0 ? acc[1] : acc[3]) + *(LAS f32x4*)(xs + 1024);
    o[2] = acc[4] + *(LAS f32x4*)(xs + 2048);
  }
  float ms = ((pmv[0] + pmv[1]) + (pmv[2] + pmv[3])) + ((pmv[4] + pmv[5]) + (pmv[6] + pmv[7]));
  ms += __shfl_xor(ms, 16);
  ms += __shfl_xor(ms, 32);
  const float mean2 = mean1 + ms;
  if (j == 0 && kh == 0 && fq == 0) ((float*)(ws + WS_MEAN2))[tok] = mean2;
  float ssq = 0.f;
  char* hcf = ws + WS_HCF + ((size_t)(tok >> 8) * 18 * 16 + ((tok >> 4) & 15)) * 1024 + fr * 16;
#pragma unroll
  for (int i = 0; i < 3; ++i) {
    const bool valid = (i < 2) || (kh == 0 && fq < 2);
    const int sub = i < 2 ? 2 * kh + i : 4;
    float v[4];
    v[0] = bflo(xv[i][0]) + o[i][0] - mean2;
    v[1] = bfhi(xv[i][0]) + o[i][1] - mean2;
    v[2] = bflo(xv[i][1]) + o[i][2] - mean2;
    v[3] = bfhi(xv[i][1]) + o[i][3] - mean2;
    const u32x2 pkd = {pk2(v[0], v[1]), pk2(v[2], v[3])};
    if (valid) {
      const float a0 = bflo(pkd[0]), a1 = bfhi(pkd[0]), a2 = bflo(pkd[1]), a3 = bfhi(pkd[1]);
      ssq += (a0 * a0 + a1 * a1) + (a2 * a2 + a3 * a3);
      const int cgi = 18 * j + 4 * sub + fq;
      *(u32x2*)(hcf + (size_t)(cgi >> 3) * 16 * 1024 + (cgi & 3) * 256 + ((cgi >> 2) & 1) * 8) = pkd;
    }
  }
  ssq += __shfl_xor(ssq, 16);
  ssq += __shfl_xor(ssq, 32);
  if (fq == 0) ((float*)(ws + WS_SSQ))[(size_t)(2 * j + kh) * 2048 + tok] = ssq;
}

constexpr int STG_B0 = 16384, STG_B1 = 16384 + 9216 + 16, STG = STG_B1 + 9216;
constexpr int K3_SSQ = 4 * STG;
constexpr int K3_RS = K3_SSQ + 16384;
constexpr int K3_LDS = K3_RS + 1024;

__global__ __launch_bounds__(512, 2) void k3_gateup(const u16* __restrict__ wg, const u16* __restrict__ wu, char* __restrict__ ws) {
  __shared__ __attribute__((aligned(16))) char smem[K3_LDS];
  const lptr L = (lptr)smem;
  const unsigned lds0 = (unsigned)(uintptr_t)L;
  const int tid = threadIdx.x, lane = tid & 63, w = __builtin_amdgcn_readfirstlane(tid >> 6);
  const int fr = lane & 15, fq = lane >> 4;
  const int xcd = blockIdx.x & 7, r = blockIdx.x >> 3;
  const int mt = (xcd >> 2) * 4 + (r >> 3), fg = (xcd & 3) * 8 + (r & 7);
  const int wm = w & 3, wn = w >> 2;

  const char* srcA = ws + WS_HCF + (size_t)mt * 18 * 16384 + w * 2048;
  const char* srcG = (const char*)wg + fg * 288;
  const char* srcU = (const char*)wu + fg * 288;
  const char* src8 = (w == 0) ? srcG : srcU;
  const unsigned vl = lane * 16;
  unsigned vb, vb8;
  {
    const int c = w * 64 + lane, c8 = 512 + lane;
    vb = (unsigned)((c / 18) * 9216 + (c % 18) * 16);
    vb8 = (unsigned)((c8 / 18) * 9216 + (c8 % 18) * 16);
  }
  const unsigned dA = lds0 + w * 2048, dG = lds0 + STG_B0 + w * 1024, dU = lds0 + STG_B1 + w * 1024;
  const unsigned d8 = lds0 + (w == 0 ? STG_B0 : STG_B1) + 8192;
  auto issueA = [&](int s) {
    const unsigned so = (s & 3) * STG;
    glds(srcA + (size_t)s * 16384, vl, dA + so);
    glds(srcA + (size_t)s * 16384 + 1024, vl, dA + so + 1024);
  };
  auto issueB = [&](int s) {
    const unsigned so = (s & 3) * STG;
    glds(srcG + (size_t)s * 32 * 9216, vb, dG + so);
    glds(srcU + (size_t)s * 32 * 9216, vb, dU + so);
    if (w < 2) glds(src8 + (size_t)s * 32 * 9216, vb8, d8 + so);
  };
  {
    const char* sq = ws + WS_SSQ + ((size_t)(2 * w) * 2048 + mt * 256) * 4;
    glds(sq, vl, lds0 + K3_SSQ + 2 * w * 1024);
    glds(sq + 8192, vl, lds0 + K3_SSQ + 2 * w * 1024 + 1024);
  }
#pragma unroll
  for (int s = 0; s < 4; ++s) { issueB(s); issueA(s); }

  const int q4 = (lane & 15) >> 2, pp = lane & 3;
  const int rowb = (4 * fq + q4) * 288;
  const int bB = rowb + 128 * wn + 8 * pp;
  const int bX = rowb + (pp < 2 ? STG_B0 : STG_B1) + 256 + 16 * wn + 8 * (pp & 1);
  const int bA = (wm * 4) * 1024 + lane * 16;

  f32x4 acc[9][4];
#pragma unroll
  for (int n = 0; n < 9; ++n)
#pragma unroll
    for (int m = 0; m < 4; ++m) acc[n][m] = (f32x4){0.f, 0.f, 0.f, 0.f};
  bf16x8 A[2][4], B[9];
  auto rdA = [&](int s, int m) { A[s & 1][m] = ldfrag(L + (s & 3) * STG + bA + m * 1024); };
  auto rdB = [&](int s, int n) {
    const int so = (s & 3) * STG;
    if (n < 8) {
      const int off = so + ((n & 1) ? STG_B1 : STG_B0) + 32 * (n >> 1);
      B[n] = cat8(trd(L + bB + off), trd(L + bB + off + 4608));
    } else {
      B[n] = cat8(trd(L + bX + so), trd(L + bX + so + 4608));
    }
  };
  auto grp = [&](int s, int n) {
#pragma unroll
    for (int m = 0; m < 4; ++m) acc[n][m] = MFMA16(B[n], A[s & 1][m], acc[n][m]);
  };

  wait_bar<12>();
#pragma unroll
  for (int m = 0; m < 4; ++m) rdA(0, m);
#pragma unroll
  for (int n = 0; n < 8; ++n) rdB(0, n);
  if (tid < 256) {
    float s = 0.f;
#pragma unroll
    for (int i = 0; i < 16; ++i) s += *(LAS float*)(L + K3_SSQ + i * 1024 + tid * 4);
    *(LAS float*)(L + K3_RS + tid * 4) = __builtin_amdgcn_rsqf(s * (1.f / 576.f) + 1e-5f);
  }
  SBAR();
#pragma unroll
  for (int s = 0; s < 18; ++s) {
    const bool nx = s < 17;
    grp(s, 0); rdB(s, 8); SBAR();
    grp(s, 1); SBAR();
    grp(s, 2); SBAR();
    if (nx) {
      if (s <= 14) wait_bar<8>(); else if (s == 15) wait_bar<4>(); else wait_bar<0>();
    }
    grp(s, 3);
    if (s + 4 < 18) issueB(s + 4);
    if (nx) { rdA(s + 1, 0); rdA(s + 1, 1); rdB(s + 1, 0); }
    SBAR();
    grp(s, 4);
    if (s + 4 < 18) issueA(s + 4);
    if (nx) { rdA(s + 1, 2); rdA(s + 1, 3); rdB(s + 1, 1); }
    SBAR();
    grp(s, 5); if (nx) { rdB(s + 1, 2); rdB(s + 1, 3); } SBAR();
    grp(s, 6); if (nx) { rdB(s + 1, 4); rdB(s + 1, 5); } SBAR();
    grp(s, 7); if (nx) { rdB(s + 1, 6); } SBAR();
    grp(s, 8); if (nx) { rdB(s + 1, 7); } SBAR();
  }
  const int mh = mt * 2 + (wm >> 1), pg = fg >> 1, odd = fg & 1;
  char* hb = ws + WS_HHF + ((size_t)(mh * 144 + pg * 9) * 8 + (wm & 1) * 4) * 1024;
  float rs[4], rn[4];
#pragma unroll
  for (int m = 0; m < 4; ++m) {
    rs[m] = *(LAS float*)(L + K3_RS + (wm * 64 + m * 16 + fr) * 4);
    rn[m] = rs[m] * -1.4426950408889634f;
  }
  auto glu = [&](float g, float u, int m) {
    const float e = __builtin_amdgcn_exp2f(g * rn[m]);
    return __builtin_amdgcn_rcpf(1.f + e) * (u * rs[m]);
  };
#pragma unroll
  for (int pr = 0; pr < 2; ++pr) {
    char* dst = hb + (size_t)(odd * 4 + 2 * wn + pr) * 8192 + lane * 16;
#pragma unroll
    for (int m = 0; m < 4; ++m) {
      const f32x4 g0 = acc[4 * pr][m], u0 = acc[4 * pr + 1][m], g1 = acc[4 * pr + 2][m], u1 = acc[4 * pr + 3][m];
      u32x4 o;
      o[0] = pk2(glu(g0[0], u0[0], m), glu(g0[1], u0[1], m));
      o[1] = pk2(glu(g0[2], u0[2], m), glu(g0[3], u0[3], m));
      o[2] = pk2(glu(g1[0], u1[0], m), glu(g1[1], u1[1], m));
      o[3] = pk2(glu(g1[2], u1[2], m), glu(g1[3], u1[3], m));
      *(u32x4*)(dst + m * 1024) = o;
    }
  }
  {
    const bool up = lane >= 32;
#pragma unroll
    for (int mp = 0; mp < 2; ++mp) {
      float g[4], u[4];
#pragma unroll
      for (int rr = 0; rr < 4; ++rr) {
        auto sw = __builtin_amdgcn_permlane32_swap(__float_as_uint(acc[8][2 * mp][rr]), __float_as_uint(acc[8][2 * mp + 1][rr]), false, false);
        g[rr] = __uint_as_float(sw[0]);
        u[rr] = __uint_as_float(sw[1]);
      }
      const float rsx = up ? rs[2 * mp + 1] : rs[2 * mp];
      const float rnx = rsx * -1.4426950408889634f;
      float hv[4];
#pragma unroll
      for (int rr = 0; rr < 4; ++rr) hv[rr] = __builtin_amdgcn_rcpf(1.f + __builtin_amdgcn_exp2f(g[rr] * rnx)) * (u[rr] * rsx);
      const int m = 2 * mp + (up ? 1 : 0);
      char* dst = hb + (size_t)8 * 8192 + m * 1024 + (fr + 16 * (2 * wn + (fq & 1))) * 16 + odd * 8;
      *(u32x2*)dst = (u32x2){pk2(hv[0], hv[1]), pk2(hv[2], hv[3])};
    }
  }
}

constexpr int K4_LDS = 4 * STG;

template <int NS, int SB>
DI void k4_body(const lptr L, const unsigned lds0, const char* srcA0, const char* srcA1, const char* wd, int nt, int s4, int w, int lane, int kg,
                int wm, int wn, char* part) {
  const int fr = lane & 15, fq = lane >> 4;
  (void)fr;
  const unsigned vl = lane * 16;
  unsigned vb, vb8, vbL, vb8L;
  {
    const int c = w * 64 + lane, c8 = 512 + lane;
    vb = (unsigned)((c / 18) * 1152 + (c % 18) * 16);
    vb8 = (unsigned)((c8 / 18) * 1152 + (c8 % 18) * 16);
    vbL = vb + ((c / 18) >= 16 ? 128 * 1152 : 0);
    vb8L = vb8 + 128 * 1152;
  }
  const char* srcB0 = wd + (size_t)(s4 * 1152) * 1152 + nt * 288;
  const char* srcB1 = srcB0 + (size_t)576 * 1152;
  const char* src8 = (w == 0) ? srcB0 : srcB1;
  const unsigned dA = lds0 + w * 1024, dB0 = lds0 + STG_B0 + w * 1024, dB1 = lds0 + STG_B1 + w * 1024;
  const unsigned d8 = lds0 + (w == 0 ? STG_B0 : STG_B1) + 8192;
  auto issue = [&](int s) {
    const unsigned so = (s & 3) * STG;
    const int i = s % 9, pgl = s / 9;
    const int rowb = pgl * 288 + (i < 4 ? 32 * i : (i < 8 ? 144 + 32 * (i - 4) : 128));
    glds(srcB0 + (size_t)rowb * 1152, i == 8 ? vbL : vb, dB0 + so);
    glds(srcB1 + (size_t)rowb * 1152, i == 8 ? vbL : vb, dB1 + so);
    if (w < 2) glds(src8 + (size_t)rowb * 1152, i == 8 ? vb8L : vb8, d8 + so);
    glds(srcA0 + (size_t)s * 8192, vl, dA + so);
    glds(srcA1 + (size_t)s * 8192, vl, dA + so + 8192);
  };
#pragma unroll
  for (int s = 0; s < 4; ++s) issue(s);
  const int q4 = (lane & 15) >> 2, pp = lane & 3;
  const int bB = (kg ? STG_B1 : STG_B0) + (4 * fq + q4) * 288 + 32 * SB + 8 * pp;
  const int bA = kg * 8192 + wm * 4096 + lane * 16;
  f32x4 acc[NS][4];
#pragma unroll
  for (int n = 0; n < NS; ++n)
#pragma unroll
    for (int m = 0; m < 4; ++m) acc[n][m] = (f32x4){0.f, 0.f, 0.f, 0.f};
  bf16x8 A[2][4], B[2][NS];
  auto rd = [&](int s) {
    const int so = (s & 3) * STG;
#pragma unroll
    for (int m = 0; m < 4; ++m) A[s & 1][m] = ldfrag(L + so + bA + m * 1024);
#pragma unroll
    for (int n = 0; n < NS; ++n) B[s & 1][n] = cat8(trd(L + so + bB + 32 * n), trd(L + so + bB + 32 * n + 4608));
  };
  wait_bar<12>();
  rd(0);
#pragma unroll
  for (int s = 0; s < 18; ++s) {
    if (s < 17) {
      if (s <= 14) wait_bar<8>(); else if (s == 15) wait_bar<4>(); else wait_bar<0>();
      if (s + 4 < 18) issue(s + 4);
      rd(s + 1);
    }
#pragma unroll
    for (int n = 0; n < NS; ++n)
#pragma unroll
      for (int m = 0; m < 4; ++m) acc[n][m] = MFMA16(B[s & 1][n], A[s & 1][m], acc[n][m]);
    SBAR();
  }
  lds_bar();
  {
    const lptr xs = L + ((wm * 2 + wn) * 2 + kg) * 10240 + lane * 16;
#pragma unroll
    for (int n = 0; n < NS; ++n)
#pragma unroll
      for (int mm = 0; mm < 2; ++mm) *(LAS f32x4*)(xs + (n * 2 + mm) * 1024) = kg ? acc[n][mm] : acc[n][2 + mm];
  }
  lds_bar();
  {
    const lptr xs = L + ((wm * 2 + wn) * 2 + (1 - kg)) * 10240 + lane * 16;
#pragma unroll
    for (int n = 0; n < NS; ++n)
#pragma unroll
      for (int mm = 0; mm < 2; ++mm) {
        const f32x4 v = (kg ? acc[n][2 + mm] : acc[n][mm]) + *(LAS f32x4*)(xs + (n * 2 + mm) * 1024);
        *(f32x4*)(part + ((size_t)(wm * 4 + kg * 2 + mm) * 36 + SB + n) * 1024 + lane * 16) = v;
      }
  }
}

__global__ __launch_bounds__(512, 2) void k4_down(const u16* __restrict__ wd, char* __restrict__ ws) {
  __shared__ __attribute__((aligned(16))) char smem[K4_LDS];
  const lptr L = (lptr)smem;
  const unsigned lds0 = (unsigned)(uintptr_t)L;
  const int tid = threadIdx.x, lane = tid & 63, w = __builtin_amdgcn_readfirstlane(tid >> 6);
  const int xcd = blockIdx.x & 7, r = blockIdx.x >> 3;
  const int s4 = xcd & 3, mt = (xcd >> 2) * 4 + (r >> 3), mh = mt * 2 + ((r >> 2) & 1), nt = r & 3;
  const int kg = w >> 2, wm = w & 1, wn = ((w >> 1) & 1) ^ kg;
  const char* srcA0 = ws + WS_HHF + ((size_t)(mh * 144 + s4 * 36) * 8 + w) * 1024;
  const char* srcA1 = srcA0 + (size_t)18 * 8192;
  char* part = ws + WS_PART + ((size_t)(s4 * 128 + mh * 8) * 36 + nt * 9) * 1024;
  if (wn == 0) k4_body<5, 0>(L, lds0, srcA0, srcA1, (const char*)wd, nt, s4, w, lane, kg, wm, wn, part);
  else k4_body<4, 5>(L, lds0, srcA0, srcA1, (const char*)wd, nt, s4, w, lane, kg, wm, wn, part);
}

__global__ __launch_bounds__(256) void k5_reduce(const char* __restrict__ ws, u16* __restrict__ out) {
  const int lane = threadIdx.x & 63, blk = blockIdx.x * 4 + (threadIdx.x >> 6);
  const int ms = blk / 36, sub = blk % 36, fr = lane & 15, fq = lane >> 4;
  const char* p = ws + WS_PART + (size_t)blk * 1024 + lane * 16;
  const size_t ps = (size_t)128 * 36 * 1024;
  const f32x4 v = (*(const f32x4*)p + *(const f32x4*)(p + ps)) + (*(const f32x4*)(p + 2 * ps) + *(const f32x4*)(p + 3 * ps));
  const int tok = ms * 16 + fr;
  const u32x2 hc = *(const u32x2*)(ws + WS_HCF + (((size_t)(ms >> 4) * 18 + (sub >> 1)) * 16 + (ms & 15)) * 1024 + lane * 16 + (sub & 1) * 8);
  const float mu = ((const float*)(ws + WS_MEAN2))[tok];
  const u32x2 o = {pk2(v[0] + (bflo(hc[0]) + mu), v[1] + (bfhi(hc[0]) + mu)), pk2(v[2] + (bflo(hc[1]) + mu), v[3] + (bfhi(hc[1]) + mu))};
  *(u32x2*)(out + (size_t)tok * 576 + sub * 16 + 4 * fq) = o;
}

extern "C" void kernel_launch(void* const* d_in, const int* in_sizes, int n_in, void* d_out, int out_size, void* d_ws, size_t ws_size,
                              hipStream_t stream) {
  const u16* x = (const u16*)d_in[0];
  const u16* wqkv = (const u16*)d_in[1];
  const u16* wo = (const u16*)d_in[2];
  const u16* wg = (const u16*)d_in[4];
  const u16* wu = (const u16*)d_in[5];
  const u16* wd = (const u16*)d_in[6];
  char* ws = (char*)d_ws;
  k1_qkv<<<256, 256, 0, stream>>>(x, wqkv, ws);
  k1b_fold<<<256, 256, 0, stream>>>(wo, ws);
  k2_wo<<<256, 512, 0, stream>>>(x, ws);
  k3_gateup<<<256, 512, 0, stream>>>(wg, wu, ws);
  k4_down<<<256, 512, 0, stream>>>(wd, ws);
  k5_reduce<<<1152, 256, 0, stream>>>(ws, (u16*)d_out);
}
```

```cpp
#include <hip/hip_runtime.h>
#include <hip/hip_bf16.h>
#include <stdint.h>

#define STPOL 2
#define LDPOL 0
typedef unsigned short u16;
typedef short bf16x8 __attribute__((ext_vector_type(8)));
typedef short s16x4 __attribute__((ext_vector_type(4)));
typedef float f32x4 __attribute__((ext_vector_type(4)));
typedef float f32x2 __attribute__((ext_vector_type(2)));
typedef unsigned u32x4 __attribute__((ext_vector_type(4)));
typedef unsigned u32x2 __attribute__((ext_vector_type(2)));
typedef __bf16 bf16x2_t __attribute__((ext_vector_type(2)));
#define LAS __attribute__((address_space(3)))
#define DI __device__ __forceinline__
typedef LAS char* lptr;

#define MFMA16(a, b, c) __builtin_amdgcn_mfma_f32_16x16x32_bf16((a), (b), (c), 0, 0, 0)
#define SBAR() __builtin_amdgcn_sched_barrier(0)
#define MFMA_ACC(c, a, b) asm("v_mfma_f32_16x16x32_bf16 %0, %1, %2, %0" : "+v"(c) : "v"(a), "v"(b))
#define MFMA_NEW(c, a, b) asm("v_mfma_f32_16x16x32_bf16 %0, %1, %2, 0" : "=&v"(c) : "v"(a), "v"(b))

constexpr int S_ = 2048, D_ = 576, F_ = 4608;

constexpr size_t WS_PQG = 0;
constexpr size_t WS_KVP = WS_PQG + 16 * 9 * 2048 * 8;
constexpr size_t WS_MEAN1 = WS_KVP + 16 * 32 * 352 * 4;
constexpr size_t WS_AF = WS_MEAN1 + 2048 * 4;
constexpr size_t WS_WEF = WS_AF + 2048 * 576 * 2;
constexpr size_t WS_PM = WS_WEF + 8 * 18 * 4608;
constexpr size_t WS_HCF = WS_PM + 32 * 2048 * 4;
constexpr size_t WS_SSQ = WS_HCF + 2048 * 576 * 2;
constexpr size_t WS_MEAN2 = WS_SSQ + 16 * 2048 * 4;
constexpr size_t WS_PART = WS_MEAN2 + 2048 * 4;
constexpr size_t WS_END = WS_PART + (size_t)16 * 2048 * 576 * 2;

template <int I> struct IC { static constexpr int v = I; };
template <int I, int N, class Fn> DI void static_for(Fn&& f) {
  if constexpr (I < N) { f(IC<I>{}); static_for<I + 1, N>(f); }
}
DI unsigned pk2(float a, float b) {
  bf16x2_t v = {(__bf16)a, (__bf16)b};
  return __builtin_bit_cast(unsigned, v);
}
DI float bflo(unsigned w) { return __uint_as_float(w << 16); }
DI float bfhi(unsigned w) { return __uint_as_float(w & 0xffff0000u); }
DI s16x4 trd(lptr p) { return __builtin_bit_cast(s16x4, __builtin_amdgcn_ds_read_tr16_b64_v4i16((LAS s16x4*)p)); }
DI bf16x8 cat8(s16x4 lo, s16x4 hi) { return __builtin_shufflevector(lo, hi, 0, 1, 2, 3, 4, 5, 6, 7); }
DI bf16x8 ldfrag(lptr p) { return *(LAS bf16x8*)p; }
DI void glds(const void* sbase, unsigned voff, unsigned lds_dst) {
  asm volatile("s_mov_b32 m0, %2\n\ts_nop 0\n\tglobal_load_lds_dwordx4 %0, %1" ::"v"(voff), "s"(sbase), "s"(lds_dst) : "memory");
}
#define WAIT_BAR(N) asm volatile("s_waitcnt vmcnt(" #N ") lgkmcnt(0)\n\ts_barrier" ::: "memory")
template <int N> DI void wait_bar() {
  asm volatile("s_waitcnt vmcnt(%0) lgkmcnt(0)\n\ts_barrier" ::"i"(N) : "memory");
}
DI void lds_bar() { asm volatile("s_waitcnt lgkmcnt(0)\n\ts_barrier" ::: "memory"); }
#define STAMP_DECL
#define STAMP(i) do {} while (0)
#define STAMP_FLUSH(kid) do {} while (0)
DI float phi_f(float t) { return (t > 0.f ? t + 1.f : __builtin_amdgcn_exp2f(t * 1.4426950408889634f)) + 1e-6f; }

constexpr int K1_PITCH = 240;
constexpr int K1_W = 576 * K1_PITCH;
constexpr int K1_ONES = K1_W;
constexpr int K1_STAT = K1_ONES + 16;
constexpr int K1_CS = K1_STAT + 1024;
constexpr int K1_LDS = K1_CS + 512;

template <int KB> DI void k1_issue(const char* xa0, const char* xa1, bf16x8 (&a)[18][2], const char* wq, const unsigned (&voff)[4],
                                   unsigned ldsw, int w) {
  asm volatile("global_load_dwordx4 %0, %1, off offset:%2" : "=v"(a[2 * KB][0]) : "v"(xa0), "i"(KB * 128) : "memory");
  asm volatile("global_load_dwordx4 %0, %1, off offset:%2" : "=v"(a[2 * KB][1]) : "v"(xa1), "i"(KB * 128) : "memory");
  asm volatile("global_load_dwordx4 %0, %1, off offset:%2" : "=v"(a[2 * KB + 1][0]) : "v"(xa0), "i"(KB * 128 + 64) : "memory");
  asm volatile("global_load_dwordx4 %0, %1, off offset:%2" : "=v"(a[2 * KB + 1][1]) : "v"(xa1), "i"(KB * 128 + 64) : "memory");
  const char* src = wq + (size_t)KB * 64 * 3456;
  const unsigned dst = ldsw + KB * 64 * K1_PITCH;
  glds(src, voff[0], dst);
  glds(src, voff[1], dst + 4096);
  glds(src, voff[2], dst + 8192);
  if (w < 3) glds(src, voff[3], dst + 12288);
}
template <int N, int KB> DI void k1_wait(bf16x8 (&a)[18][2]) {
  asm volatile("s_waitcnt vmcnt(%4) lgkmcnt(0)\n\ts_barrier"
               : "+v"(a[2 * KB][0]), "+v"(a[2 * KB][1]), "+v"(a[2 * KB + 1][0]), "+v"(a[2 * KB + 1][1])
               : "i"(N)
               : "memory");
}

__global__ __launch_bounds__(256, 1) void k1_qkv(const u16* __restrict__ x, const u16* __restrict__ wqkv, char* __restrict__ ws) {
  __shared__ __attribute__((aligned(16))) char smem[K1_LDS];
  STAMP_DECL; STAMP(0);
  const lptr L = (lptr)smem;
  const unsigned lds0 = (unsigned)(uintptr_t)L;
  const int tid = threadIdx.x, lane = tid & 63, w = __builtin_amdgcn_readfirstlane(tid >> 6);
  const int fr = lane & 15, fq = lane >> 4;
  const int xcd = blockIdx.x & 7, r = blockIdx.x >> 3;
  const int tt = (xcd & 3) * 4 + (r & 3), p = (xcd >> 2) * 8 + (r >> 2);
  const int o8 = (p & 1) * 8;
  const int tok0 = tt * 128 + w * 32;

  unsigned voff[4];
#pragma unroll
  for (int i = 0; i < 4; ++i) {
    int pi = w + 4 * i;
    if (pi > 14) pi = 14;
    const int c = pi * 64 + lane, row = c / 15, cc = c % 15, seg = cc / 5, ch = cc % 5;
    const int b = row >> 5, rho = row & 31, t = rho >> 4, rr = rho & 15;
    const int g = 2 * (rr & 1) + (rr >> 3), q = (rr >> 1) & 3;
    const int k = 32 * b + 8 * g + 4 * t + q;
    voff[i] = (unsigned)(k * 3456 + seg * 1152 + 72 * p - o8 + ch * 16);
  }
  const unsigned ldsw = lds0 + w * 1024;
  const char* xa0 = (const char*)x + (size_t)(tok0 + fr) * 1152 + fq * 16;
  const char* xa1 = xa0 + 16 * 1152;
  bf16x8 a[18][2];
  k1_issue<0>(xa0, xa1, a, (const char*)wqkv, voff, ldsw, w);
  k1_issue<1>(xa0, xa1, a, (const char*)wqkv, voff, ldsw, w);
  k1_issue<2>(xa0, xa1, a, (const char*)wqkv, voff, ldsw, w);
  if (tid == 0) *(LAS u32x4*)(L + K1_ONES) = (u32x4){0x3F803F80u, 0x3F803F80u, 0x3F803F80u, 0x3F803F80u};

  const int q4 = (lane & 15) >> 2, pp = lane & 3;
  const int rowb = (2 * (4 * (fq & 1) + q4) + (fq >> 1)) * K1_PITCH;
  const int bq = rowb + o8 + 8 * pp;
  int bx = pp < 3 ? rowb + 80 * pp + o8 + 64 : K1_ONES;
  const int bxs = pp < 3 ? 32 * K1_PITCH : 0, bxt = pp < 3 ? 16 * K1_PITCH : 0;
  const int sA = w, sB = (w + 4 < 7) ? w + 4 : 3;
  const int bcA = rowb + o8 + (sA >> 1) * 80 + 32 * (sA & 1) + 8 * pp;
  const int bcB = (sB == 6) ? rowb + 80 * (pp < 3 ? pp : 0) + o8 + 64 : rowb + o8 + (sB >> 1) * 80 + 32 * (sB & 1) + 8 * pp;

  f32x4 acc[7][2], gram[2], cs[2];
#pragma unroll
  for (int i = 0; i < 7; ++i) acc[i][0] = acc[i][1] = (f32x4){0.f, 0.f, 0.f, 0.f};
  gram[0] = gram[1] = cs[0] = cs[1] = (f32x4){0.f, 0.f, 0.f, 0.f};
  const bf16x8 ones = {0x3F80, 0x3F80, 0x3F80, 0x3F80, 0x3F80, 0x3F80, 0x3F80, 0x3F80};

  bf16x8 B[2][9];
  auto readB = [&](int s, bf16x8(&b)[9]) {
    const int so = s * 32 * K1_PITCH;
#pragma unroll
    for (int i = 0; i < 6; ++i) {
      const int off = so + (i >> 1) * 80 + 32 * (i & 1);
      b[i] = cat8(trd(L + bq + off), trd(L + bq + off + 16 * K1_PITCH));
    }
    b[6] = cat8(trd(L + bx), trd(L + bx + bxt));
    bx += bxs;
    b[7] = cat8(trd(L + bcA + so), trd(L + bcA + so + 16 * K1_PITCH));
    b[8] = cat8(trd(L + bcB + so), trd(L + bcB + so + 16 * K1_PITCH));
  };
  auto mma = [&](const bf16x8(&av)[2], const bf16x8(&b)[9]) {
#pragma unroll
    for (int m = 0; m < 2; ++m) {
      acc[0][m] = MFMA16(b[0], av[m], acc[0][m]);
      acc[1][m] = MFMA16(b[1], av[m], acc[1][m]);
#pragma unroll
      for (int i = 2; i < 7; ++i) acc[i][m] = MFMA16(av[m], b[i], acc[i][m]);
      gram[m] = MFMA16(av[m], av[m], gram[m]);
    }
    cs[0] = MFMA16(ones, b[7], cs[0]);
    cs[1] = MFMA16(ones, b[8], cs[1]);
  };

  STAMP(1);
  k1_wait<14, 0>(a);
  STAMP(2);
  readB(0, B[0]);
#define K1_PAIR(KB, NW)                                                                        \
  readB(2 * KB + 1, B[1]);                                                                     \
  mma(a[2 * KB], B[0]);                                                                        \
  if (KB < 8) {                                                                                \
    if (KB + 3 <= 8) k1_issue<(KB + 3 <= 8 ? KB + 3 : 8)>(xa0, xa1, a, (const char*)wqkv, voff, ldsw, w); \
    k1_wait<NW, (KB < 8 ? KB + 1 : 8)>(a);                                                     \
    readB(2 * KB + 2, B[0]);                                                                   \
  }                                                                                            \
  mma(a[2 * KB + 1], B[1]);
  K1_PAIR(0, 14) K1_PAIR(1, 14) K1_PAIR(2, 14) K1_PAIR(3, 14) K1_PAIR(4, 14) K1_PAIR(5, 14) K1_PAIR(6, 7) K1_PAIR(7, 0) K1_PAIR(8, 0)
#undef K1_PAIR

  STAMP(3);
  LAS float* st = (LAS float*)(L + K1_STAT + w * 256);
#pragma unroll
  for (int m = 0; m < 2; ++m) {
    if ((fr >> 2) == fq) {
      const int rr = fr & 3;
      const float g = rr == 0 ? gram[m][0] : rr == 1 ? gram[m][1] : rr == 2 ? gram[m][2] : gram[m][3];
      st[(16 * m + fr) * 2 + 1] = g;
    }
    if (fr == 12) {
#pragma unroll
      for (int rr = 0; rr < 4; ++rr) st[(16 * m + 4 * fq + rr) * 2] = acc[6][m][rr];
    }
  }
  LAS float* csl = (LAS float*)(L + K1_CS);
  if (lane < 16) {
    csl[sA * 16 + lane] = cs[0][0];
    csl[(w + 4) * 16 + lane] = cs[1][0];
  }
  lds_bar();
  float meanT[2], rstdT[2];
  float meanN[2][4], rstdN[2][4];
#pragma unroll
  for (int m = 0; m < 2; ++m) {
    {
      const f32x2 v = *(LAS f32x2*)(st + (16 * m + fr) * 2);
      const float mu = v[0] * (1.f / 576.f);
      const float var = fmaxf(v[1] * (1.f / 576.f) - mu * mu, 0.f);
      meanT[m] = mu;
      rstdT[m] = __builtin_amdgcn_rsqf(var + 1e-5f);
    }
#pragma unroll
    for (int rr = 0; rr < 4; ++rr) {
      const f32x2 v = *(LAS f32x2*)(st + (16 * m + 4 * fq + rr) * 2);
      const float mu = v[0] * (1.f / 576.f);
      const float var = fmaxf(v[1] * (1.f / 576.f) - mu * mu, 0.f);
      meanN[m][rr] = mu;
      rstdN[m][rr] = __builtin_amdgcn_rsqf(var + 1e-5f);
    }
  }
  if (p == 0 && fq == 0) {
    float* mean1 = (float*)(ws + WS_MEAN1);
    mean1[tok0 + fr] = meanT[0];
    mean1[tok0 + 16 + fr] = meanT[1];
  }
  char* pqg = ws + WS_PQG + (size_t)p * 9 * 2048 * 8;
#pragma unroll
  for (int i = 0; i < 2; ++i) {
    const f32x4 c4 = *(LAS f32x4*)(csl + i * 16 + 4 * fq);
#pragma unroll
    for (int m = 0; m < 2; ++m) {
      float v[4];
#pragma unroll
      for (int rr = 0; rr < 4; ++rr) v[rr] = phi_f((acc[i][m][rr] - meanT[m] * c4[rr]) * rstdT[m]);
      u32x2 o = {pk2(v[0], v[1]), pk2(v[2], v[3])};
      *(u32x2*)(pqg + ((size_t)(4 * i + fq) * 2048 + tok0 + 16 * m + fr) * 8) = o;
    }
  }
  bf16x8 fragv[5];
#pragma unroll
  for (int i = 2; i < 7; ++i) {
    const float c1 = csl[i * 16 + fr];
    float v[2][4];
#pragma unroll
    for (int m = 0; m < 2; ++m)
#pragma unroll
      for (int rr = 0; rr < 4; ++rr) {
        float t = (acc[i][m][rr] - meanN[m][rr] * c1) * rstdN[m][rr];
        if (i < 4) t = phi_f(t);
        if (i == 6) t = fr < 8 ? phi_f(t) : (fr < 12 ? t : 1.0f);
        v[m][rr] = t;
      }
    u32x4 pkd = {pk2(v[0][0], v[0][1]), pk2(v[0][2], v[0][3]), pk2(v[1][0], v[1][1]), pk2(v[1][2], v[1][3])};
    fragv[i - 2] = __builtin_bit_cast(bf16x8, pkd);
    if (i == 6 && fr < 4) {
      u16* dst = (u16*)(pqg + ((size_t)8 * 2048 + tok0 + 4 * fq) * 8) + fr;
#pragma unroll
      for (int m = 0; m < 2; ++m) {
        dst[(16 * m + 0) * 4] = (u16)(pkd[2 * m] & 0xffff);
        dst[(16 * m + 1) * 4] = (u16)(pkd[2 * m] >> 16);
        dst[(16 * m + 2) * 4] = (u16)(pkd[2 * m + 1] & 0xffff);
        dst[(16 * m + 3) * 4] = (u16)(pkd[2 * m + 1] >> 16);
      }
    }
  }
  const f32x4 z4 = {0.f, 0.f, 0.f, 0.f};
  f32x4 T[8];
  T[0] = MFMA16(fragv[0], fragv[2], z4);
  T[1] = MFMA16(fragv[0], fragv[3], z4);
  T[2] = MFMA16(fragv[1], fragv[2], z4);
  T[3] = MFMA16(fragv[1], fragv[3], z4);
  T[4] = MFMA16(fragv[0], fragv[4], z4);
  T[5] = MFMA16(fragv[1], fragv[4], z4);
  T[6] = MFMA16(fragv[4], fragv[3], z4);
  T[7] = MFMA16(fragv[4], fragv[4], z4);
#pragma unroll
  for (int i = 0; i < 8; ++i) *(LAS f32x4*)(L + (w * 8 + i) * 1024 + lane * 16) = T[i];
  lds_bar();
  float* kvp = (float*)(ws + WS_KVP) + (size_t)(tt * 32 + 2 * p) * 352;
  for (int idx = tid; idx < 2 * 342; idx += 256) {
    const int a_ = idx >= 342, j = idx - a_ * 342;
    int d, e;
    if (j < 324) { d = j / 18; e = j - d * 18 + 18 * a_; d += 18 * a_; }
    else { d = j - 324 + 18 * a_; e = 36; }
    const int dt = d >> 4, et = e >> 4;
    const int dr = dt == 2 ? d - 32 + 4 : d & 15;
    const int ec = et == 2 ? e - 32 + 8 : e & 15;
    const int tile = et == 2 ? (dt == 2 ? 7 : 4 + dt) : (dt == 2 ? 6 : dt * 2 + et);
    const int off = tile * 1024 + (ec + 16 * (dr >> 2)) * 16 + (dr & 3) * 4;
    const float s = (*(LAS float*)(L + off) + *(LAS float*)(L + off + 8192)) + (*(LAS float*)(L + off + 16384) + *(LAS float*)(L + off + 24576));
    kvp[a_ * 352 + j] = s;
  }
  STAMP(4);
  STAMP(5); STAMP_FLUSH(0);
}

__global__ __launch_bounds__(256) void k1b_fold(const u16* __restrict__ wo, char* __restrict__ ws) {
  __shared__ __attribute__((aligned(16))) float red[2][352];
  __shared__ __attribute__((aligned(16))) float kvz[352];
  __shared__ __attribute__((aligned(16))) float rsum[32];
  __shared__ __attribute__((aligned(16))) float rm[32];
  STAMP_DECL; STAMP(0);
  const int tid = threadIdx.x;
  const int h = blockIdx.x >> 3, j = blockIdx.x & 7;
  const int p = h >> 1, a = h & 1;
  const int tok = 256 * j + tid;
  u32x2 pq[5];
  const char* pqg = ws + WS_PQG + ((size_t)(p * 9 + 4 * a) * 2048 + tok) * 8;
#pragma unroll
  for (int g = 0; g < 5; ++g) pq[g] = *(const u32x2*)(pqg + (size_t)g * 2048 * 8);
  f32x4 s4 = {0.f, 0.f, 0.f, 0.f};
  const int c88 = tid % 88, hf = tid / 88;
  if (tid < 176) {
    const float* src = (const float*)(ws + WS_KVP) + (size_t)(hf * 8 * 32 + h) * 352 + c88 * 4;
    f32x4 v[8];
#pragma unroll
    for (int t = 0; t < 8; ++t) v[t] = *(const f32x4*)(src + (size_t)t * 32 * 352);
    s4 = ((v[0] + v[1]) + (v[2] + v[3])) + ((v[4] + v[5]) + (v[6] + v[7]));
  }
  float rs = 0.f;
  if (tid < 144) {
    const int e = tid >> 3, l8 = tid & 7;
    const char* src = (const char*)wo + (size_t)(18 * h + e) * 1152 + l8 * 16;
    u32x4 v[9];
#pragma unroll
    for (int i = 0; i < 9; ++i) v[i] = *(const u32x4*)(src + i * 128);
#pragma unroll
    for (int i = 0; i < 9; ++i)
#pragma unroll
      for (int q = 0; q < 4; ++q) rs += bflo(v[i][q]) + bfhi(v[i][q]);
  }
  rs += __shfl_xor(rs, 1);
  rs += __shfl_xor(rs, 2);
  rs += __shfl_xor(rs, 4);
  const int c72 = tid % 72, part = tid / 72;
  float wcol[18];
  if (tid < 216) {
#pragma unroll
    for (int e = 0; e < 18; ++e) wcol[e] = __uint_as_float((unsigned)wo[(size_t)(18 * h + e) * 576 + 72 * j + c72] << 16);
  }
  if (tid < 176) *(f32x4*)&red[hf][c88 * 4] = s4;
  if (tid < 144 && (tid & 7) == 0) rsum[tid >> 3] = rs;
  __syncthreads();
  STAMP(1);
  for (int i = tid; i < 352; i += 256) kvz[i] = red[0][i] + red[1][i];
  __syncthreads();
  if (tid < 18) {
    float s = 0.f;
#pragma unroll
    for (int e = 0; e < 18; ++e) s += kvz[tid * 18 + e] * rsum[e];
    rm[tid] = s * (1.f / 576.f);
  }
  if (tid < 216) {
    const int sub = c72 >> 4, frc = c72 & 15;
    char* base = ws + WS_WEF + (size_t)j * 18 * 4608;
    if (part < 2) {
      float o[8];
#pragma unroll
      for (int i = 0; i < 8; ++i) {
        float s = 0.f;
#pragma unroll
        for (int e = 0; e < 18; ++e) s += kvz[(part * 8 + i) * 18 + e] * wcol[e];
        o[i] = s;
      }
      const int ci = 2 * h + part, ks = ci >> 2, fq = ci & 3;
      const int off = ks * 4608 + (sub < 4 ? sub * 1024 + (frc + 16 * fq) * 16 : 4096 + (frc + 8 * fq) * 16);
      *(u32x4*)(base + off) = (u32x4){pk2(o[0], o[1]), pk2(o[2], o[3]), pk2(o[4], o[5]), pk2(o[6], o[7])};
    } else {
      float o[2];
#pragma unroll
      for (int i = 0; i < 2; ++i) {
        float s = 0.f;
#pragma unroll
        for (int e = 0; e < 18; ++e) s += kvz[(16 + i) * 18 + e] * wcol[e];
        o[i] = s;
      }
      const int ci = 64 + (h >> 2), ks = ci >> 2, fq = ci & 3;
      const int off = ks * 4608 + (sub < 4 ? sub * 1024 + (frc + 16 * fq) * 16 : 4096 + (frc + 8 * fq) * 16) + (h & 3) * 4;
      *(unsigned*)(base + off) = pk2(o[0], o[1]);
    }
  }
  __syncthreads();
  STAMP(2);
  const unsigned am = a ? 0xffffffffu : 0u;
  float qq[18];
#pragma unroll
  for (int i = 0; i < 9; ++i) {
    const unsigned w0 = pq[i >> 1][i & 1], w1 = pq[(i + 1) >> 1][(i + 1) & 1];
    const unsigned wv = (w1 & am) | (w0 & ~am);
    qq[2 * i] = bflo(wv);
    qq[2 * i + 1] = bfhi(wv);
  }
  float den = 0.f;
#pragma unroll
  for (int d = 0; d < 18; ++d) den += qq[d] * kvz[324 + d];
  den += 1e-6f;
  const float inv = 1.0f / den;
  unsigned pk[9];
  float pmv = 0.f;
#pragma unroll
  for (int d = 0; d < 9; ++d) {
    pk[d] = pk2(qq[2 * d] * inv, qq[2 * d + 1] * inv);
    pmv += bflo(pk[d]) * rm[2 * d] + bfhi(pk[d]) * rm[2 * d + 1];
  }
  ((float*)(ws + WS_PM))[(size_t)h * 2048 + tok] = pmv;
  const int t64 = tok >> 6, msub = (tok >> 4) & 3, fr = tok & 15;
  char* af = ws + WS_AF + (size_t)t64 * 18 * 4096 + msub * 1024;
  {
    const int ks = h >> 1, fq = 2 * (h & 1);
    char* d0 = af + ks * 4096 + (fr + 16 * fq) * 16;
    *(u32x4*)d0 = (u32x4){pk[0], pk[1], pk[2], pk[3]};
    *(u32x4*)(d0 + 256) = (u32x4){pk[4], pk[5], pk[6], pk[7]};
    const int ci = 64 + (h >> 2);
    *(unsigned*)(af + (ci >> 2) * 4096 + (fr + 16 * (ci & 3)) * 16 + (h & 3) * 4) = pk[8];
  }
  STAMP(3);
  STAMP(4); STAMP_FLUSH(1);
}

constexpr int K2_STEP = 4096 + 4608;
constexpr int K2_LDS = 18 * K2_STEP;

__global__ __launch_bounds__(512, 2) void k2_wo(const u16* __restrict__ x, char* __restrict__ ws) {
  __shared__ __attribute__((aligned(16))) char smem[K2_LDS];
  STAMP_DECL; STAMP(0);
  const lptr L = (lptr)smem;
  const unsigned lds0 = (unsigned)(uintptr_t)L;
  const int tid = threadIdx.x, lane = tid & 63, w = __builtin_amdgcn_readfirstlane(tid >> 6);
  const int fr = lane & 15, fq = lane >> 4;
  const int xcd = blockIdx.x & 7, r = blockIdx.x >> 3;
  const int t64 = (xcd & 3) * 8 + (r & 7), j = (xcd >> 2) * 4 + (r >> 3);
  const int msub = w & 3, kh = w >> 2;
  const int tok = t64 * 64 + msub * 16 + fr;

  const char* srcA = ws + WS_AF + (size_t)t64 * 18 * 4096;
  const char* srcB = ws + WS_WEF + (size_t)j * 18 * 4608;
  const unsigned vl = lane * 16;
#pragma unroll
  for (int kb = 0; kb < 9; ++kb) {
    glds(srcA + kb * 8192 + w * 1024, vl, lds0 + kb * 8192 + w * 1024);
    glds(srcB + kb * 9216 + w * 1024, vl, lds0 + 18 * 4096 + kb * 9216 + w * 1024);
    if (w == (kb & 7)) glds(srcB + kb * 9216 + 8192, vl, lds0 + 18 * 4096 + kb * 9216 + 8192);
  }
  float pmv[8];
  const float* pm = (const float*)(ws + WS_PM) + (size_t)(8 * fq) * 2048 + tok;
#pragma unroll
  for (int i = 0; i < 8; ++i) pmv[i] = pm[(size_t)i * 2048];
  const float mean1 = ((const float*)(ws + WS_MEAN1))[tok];
  u32x2 xv[3];
  const char* xr = (const char*)x + (size_t)tok * 1152 + 144 * j + 8 * fq;
  xv[0] = *(const u32x2*)(xr + kh * 64);
  xv[1] = *(const u32x2*)(xr + kh * 64 + 32);
  xv[2] = *(const u32x2*)((const char*)x + (size_t)tok * 1152 + 144 * j + 128 + 8 * (fq & 1));
  SBAR();

  f32x4 acc[5];
#pragma unroll
  for (int i = 0; i < 5; ++i) acc[i] = (f32x4){0.f, 0.f, 0.f, 0.f};
  const int aoff = kh * 4096 + msub * 1024 + lane * 16;
  const int boff = 18 * 4096 + kh * 4608 + lane * 16;
  const int b4off = 18 * 4096 + kh * 4608 + 4096 + ((fr & 7) + 8 * fq) * 16;
#define K2_STEPM(KB)                                                         \
  {                                                                          \
    wait_bar<2 * (8 - KB) + 12>();                                           \
    const bf16x8 av = ldfrag(L + aoff + KB * 8192);                          \
    _Pragma("unroll") for (int i = 0; i < 4; ++i) {                          \
      const bf16x8 bv = ldfrag(L + boff + KB * 9216 + i * 1024);             \
      acc[i] = MFMA16(bv, av, acc[i]);                                       \
    }                                                                        \
    const bf16x8 bv = ldfrag(L + b4off + KB * 9216);                         \
    acc[4] = MFMA16(bv, av, acc[4]);                                         \
  }
  STAMP(1);
  K2_STEPM(0) STAMP(2); K2_STEPM(1) K2_STEPM(2) K2_STEPM(3) K2_STEPM(4) K2_STEPM(5) K2_STEPM(6) K2_STEPM(7) K2_STEPM(8)
#undef K2_STEPM
  SBAR();
  STAMP(3);
  lds_bar();
  {
    const lptr xs = L + (msub * 2 + kh) * 3072 + lane * 16;
    if (kh == 0) {
      *(LAS f32x4*)(xs) = acc[2];
      *(LAS f32x4*)(xs + 1024) = acc[3];
    } else {
      *(LAS f32x4*)(xs) = acc[0];
      *(LAS f32x4*)(xs + 1024) = acc[1];
      *(LAS f32x4*)(xs + 2048) = acc[4];
    }
  }
  lds_bar();
  f32x4 o[3];
  {
    const lptr xs = L + (msub * 2 + (1 - kh)) * 3072 + lane * 16;
    o[0] = (kh == 0 ? acc[0] : acc[2]) + *(LAS f32x4*)(xs);
    o[1] = (kh == 0 ? acc[1] : acc[3]) + *(LAS f32x4*)(xs + 1024);
    o[2] = acc[4] + *(LAS f32x4*)(xs + 2048);
  }
  float ms = ((pmv[0] + pmv[1]) + (pmv[2] + pmv[3])) + ((pmv[4] + pmv[5]) + (pmv[6] + pmv[7]));
  ms += __shfl_xor(ms, 16);
  ms += __shfl_xor(ms, 32);
  const float mean2 = mean1 + ms;
  if (j == 0 && kh == 0 && fq == 0) ((float*)(ws + WS_MEAN2))[tok] = mean2;
  float ssq = 0.f;
  char* hcf = ws + WS_HCF + ((size_t)(tok >> 8) * 18 * 16 + ((tok >> 4) & 15)) * 1024 + fr * 16;
#pragma unroll
  for (int i = 0; i < 3; ++i) {
    const bool valid = (i < 2) || (kh == 0 && fq < 2);
    const int sub = i < 2 ? 2 * kh + i : 4;
    float v[4];
    v[0] = bflo(xv[i][0]) + o[i][0] - mean2;
    v[1] = bfhi(xv[i][0]) + o[i][1] - mean2;
    v[2] = bflo(xv[i][1]) + o[i][2] - mean2;
    v[3] = bfhi(xv[i][1]) + o[i][3] - mean2;
    const u32x2 pkd = {pk2(v[0], v[1]), pk2(v[2], v[3])};
    if (valid) {
      const float a0 = bflo(pkd[0]), a1 = bfhi(pkd[0]), a2 = bflo(pkd[1]), a3 = bfhi(pkd[1]);
      ssq += (a0 * a0 + a1 * a1) + (a2 * a2 + a3 * a3);
      const int cgi = 18 * j + 4 * sub + fq;
      *(u32x2*)(hcf + (size_t)(cgi >> 3) * 16 * 1024 + (cgi & 3) * 256 + ((cgi >> 2) & 1) * 8) = pkd;
    }
  }
  ssq += __shfl_xor(ssq, 16);
  ssq += __shfl_xor(ssq, 32);
  if (fq == 0) ((float*)(ws + WS_SSQ))[(size_t)(2 * j + kh) * 2048 + tok] = ssq;
  STAMP(4);
  STAMP(5); STAMP_FLUSH(2);
}

constexpr int F_G = 8192, F_U = 8192 + 18432 + 16, F_STG = F_U + 18432;
constexpr int F_SSQ = 3 * F_STG;
constexpr int F_RS = F_SSQ + 8192;
constexpr int F_H = 163840 - 73728;
constexpr int F_LDS = 163840;
__device__ constexpr int f_wslot(int i) { return i == 0 ? 0 : i == 1 ? F_STG : i == 2 ? F_H : F_H + 36864; }

__global__ __launch_bounds__(512, 2) void kf_ffn(const u16* __restrict__ wg, const u16* __restrict__ wu, const u16* __restrict__ wd,
                                                 char* __restrict__ ws) {
  __shared__ __attribute__((aligned(16))) char smem[F_LDS];
  STAMP_DECL; STAMP(0);
  const lptr L = (lptr)smem;
  const unsigned lds0 = (unsigned)(uintptr_t)L;
  const int tid = threadIdx.x, lane = tid & 63, w = __builtin_amdgcn_readfirstlane(tid >> 6);
  const int fr = lane & 15, fq = lane >> 4;
  const int xcd = blockIdx.x & 7, r = blockIdx.x >> 3;
  const int fs = xcd * 2 + (r & 1), mh = r >> 1;
  const int wm = w & 1, wn = w >> 1;

  const char* srcA = ws + WS_HCF + ((size_t)(mh >> 1) * 18 * 16 + (mh & 1) * 8 + w) * 1024;
  const char* srcG = (const char*)wg + fs * 576;
  const char* srcU = (const char*)wu + fs * 576;
  const char* srcX = (w < 2) ? srcG : srcU;
  const unsigned vl = lane * 16;
  unsigned vb0, vb1, vbx;
  {
    auto mk = [&](int pi) {
      const int c = pi * 64 + lane, rho = c / 36, cs = c % 36;
      return (unsigned)(rho * 9216 + ((((cs >> 1) ^ ((rho >> 2) & 1)) << 1) + (cs & 1)) * 16);
    };
    vb0 = mk(w); vb1 = mk(w + 8); vbx = mk(16 + (w & 1));
  }
  const unsigned dA = lds0 + w * 1024, dG = lds0 + F_G + w * 1024, dU = lds0 + F_U + w * 1024;
  const unsigned dX = lds0 + (w < 2 ? F_G : F_U) + (16 + (w & 1)) * 1024;
  auto issue1a = [&](int s) __attribute__((always_inline)) {
    const unsigned so = (s % 3) * F_STG;
    glds(srcG + (size_t)s * 32 * 9216, vb0, dG + so);
    glds(srcU + (size_t)s * 32 * 9216, vb0, dU + so);
    glds(srcA + (size_t)s * 16384, vl, dA + so);
  };
  auto issue1b = [&](int s) __attribute__((always_inline)) {
    const unsigned so = (s % 3) * F_STG;
    glds(srcG + (size_t)s * 32 * 9216, vb1, dG + so + 8192);
    glds(srcU + (size_t)s * 32 * 9216, vb1, dU + so + 8192);
    if (w < 4) glds(srcX + (size_t)s * 32 * 9216, vbx, dX + so);
  };
  const char* srcD = (const char*)wd + (size_t)(fs * 288 + w * 8) * 1152;
  const unsigned vd = (unsigned)((lane >> 3) * 1152 + (((((lane & 7) >> 1) ^ (fq & 3)) << 1) + (lane & 1)) * 16);
  auto issue2a = [&](int c) __attribute__((always_inline)) {
    const unsigned d = lds0 + f_wslot(c & 3) + w * 1024;
    glds(srcD + c * 128, vd, d);
    glds(srcD + c * 128 + 64 * 1152, vd, d + 8192);
  };
  auto issue2b = [&](int c) __attribute__((always_inline)) {
    const unsigned d = lds0 + f_wslot(c & 3) + w * 1024;
    glds(srcD + c * 128 + 128 * 1152, vd, d + 16384);
    glds(srcD + c * 128 + 192 * 1152, vd, d + 24576);
    if (w < 4) glds(srcD + c * 128 + 256 * 1152, vd, d + 32768);
  };
  glds(ws + WS_SSQ + ((size_t)(2 * w) * 2048 + mh * 128) * 4, (unsigned)((lane >> 5) * 8192 + (lane & 31) * 16), lds0 + F_SSQ + w * 1024);
#pragma unroll
  for (int s = 0; s < 3; ++s) { issue1a(s); issue1b(s); }

  const int q4 = (lane & 15) >> 2, pp = lane & 3, gb = fq & 1;
  const int rowb = (4 * fq + q4) * 576;
  lptr pE[2], pO[2], pX[2], pA[2];
  pE[0] = L + rowb + 128 * wn + 32 * gb + 8 * pp;
  pO[0] = L + rowb + 128 * wn - 32 * gb + 8 * pp;
  pX[0] = L + rowb + (pp < 2 ? F_G : F_U) + ((16 + (wn >> 1)) ^ gb) * 32 + 16 * (wn & 1) + 8 * (pp & 1);
  pA[0] = L + (wm * 4) * 1024 + lane * 16;
  pE[1] = pE[0] + F_STG + F_G; pO[1] = pO[0] + F_STG + F_G; pX[1] = pX[0] + F_STG + F_G; pA[1] = pA[0] + F_STG + F_G;
  asm volatile("" : "+v"(pE[1]), "+v"(pO[1]), "+v"(pX[1]), "+v"(pA[1]));
  auto adr = [&](lptr(&b)[2], int o) __attribute__((always_inline)) { return o >= F_STG + F_G ? b[1] + (o - F_STG - F_G) : b[0] + o; };

  f32x4 acc[9][4];
  bf16x8 A[2][4], B[9];
  auto rdA = [&](int s, int m) __attribute__((always_inline)) { A[s & 1][m] = ldfrag(adr(pA, (s % 3) * F_STG + m * 1024)); };
  auto rdB = [&](int s, int n) __attribute__((always_inline)) {
    const int so = (s % 3) * F_STG;
    if (n < 8) {
      const int nn = n >> 1;
      const int off = so + ((n & 1) ? F_U : F_G) + 32 * nn;
      if (nn & 1) B[n] = cat8(trd(adr(pO, off)), trd(adr(pO, off + 16 * 576)));
      else B[n] = cat8(trd(adr(pE, off)), trd(adr(pE, off + 16 * 576)));
    } else {
      B[n] = cat8(trd(adr(pX, so + F_G) - F_G), trd(adr(pX, so + F_G + 16 * 576) - F_G));
    }
  };
  auto grp = [&](int s, int n) __attribute__((always_inline)) {
#pragma unroll
    for (int m = 0; m < 4; ++m) {
      if (s == 0) MFMA_NEW(acc[n][m], B[n], A[s & 1][m]); else MFMA_ACC(acc[n][m], B[n], A[s & 1][m]);
    }
  };

  STAMP(1);
  wait_bar<10>();
  STAMP(2);
#pragma unroll
  for (int m = 0; m < 4; ++m) rdA(0, m);
#pragma unroll
  for (int n = 0; n < 8; ++n) rdB(0, n);
  if (tid < 128) {
    float s = 0.f;
#pragma unroll
    for (int i = 0; i < 16; ++i) s += *(LAS float*)(L + F_SSQ + i * 512 + tid * 4);
    *(LAS float*)(L + F_RS + tid * 4) = __builtin_amdgcn_rsqf(s * (1.f / 576.f) + 1e-5f);
  }
  SBAR();
  float rs[4];
  static_for<0, 18>([&](auto ic) {
    constexpr int s = decltype(ic)::v;
    constexpr bool nx = s < 17;
    grp(s, 0); rdB(s, 8); SBAR();
    grp(s, 1); SBAR();
    grp(s, 2); SBAR();
    if (s <= 15) wait_bar<5>(); else if (s == 16) wait_bar<4>(); else lds_bar();
    grp(s, 3);
    if (s + 3 < 18) issue1a(s + 3); else if (s < 17) issue2a(s - 15);
    if (nx) { rdA(s + 1, 0); rdA(s + 1, 1); rdB(s + 1, 0); }
    SBAR();
    grp(s, 4);
    if (s + 3 < 18) issue1b(s + 3); else if (s < 17) issue2b(s - 15);
    if (nx) { rdA(s + 1, 2); rdA(s + 1, 3); rdB(s + 1, 1); }
    SBAR();
    grp(s, 5); if (nx) { rdB(s + 1, 2); rdB(s + 1, 3); } SBAR();
    grp(s, 6); if (nx) { rdB(s + 1, 4); rdB(s + 1, 5); } SBAR();
    grp(s, 7); if (nx) { rdB(s + 1, 6); }
    if (s == 16) {
#pragma unroll
      for (int m = 0; m < 4; ++m) rs[m] = *(LAS float*)(L + F_RS + (wm * 64 + m * 16 + fr) * 4);
    }
    SBAR();
    grp(s, 8); if (nx) { rdB(s + 1, 7); } SBAR();
  });
  asm volatile("s_nop 7\n\ts_nop 1" ::: "memory");
  SBAR();
  STAMP(3);
  {
    const lptr hb = L + F_H + (wm * 4) * 1024;
    auto glu = [&](float g, float u, float rsv, float rnv) { return __builtin_amdgcn_rcpf(1.f + __builtin_amdgcn_exp2f(g * rnv)) * (u * rsv); };
#pragma unroll
    for (int pr = 0; pr < 2; ++pr) {
#pragma unroll
      for (int m = 0; m < 4; ++m) {
        const float rsv = rs[m], rnv = rsv * -1.4426950408889634f;
        const f32x4 g0 = acc[4 * pr][m], u0 = acc[4 * pr + 1][m], g1 = acc[4 * pr + 2][m], u1 = acc[4 * pr + 3][m];
        u32x4 o;
        o[0] = pk2(glu(g0[0], u0[0], rsv, rnv), glu(g0[1], u0[1], rsv, rnv));
        o[1] = pk2(glu(g0[2], u0[2], rsv, rnv), glu(g0[3], u0[3], rsv, rnv));
        o[2] = pk2(glu(g1[0], u1[0], rsv, rnv), glu(g1[1], u1[1], rsv, rnv));
        o[3] = pk2(glu(g1[2], u1[2], rsv, rnv), glu(g1[3], u1[3], rsv, rnv));
        *(LAS u32x4*)(hb + (2 * wn + pr) * 8192 + m * 1024 + lane * 16) = o;
      }
    }
    const bool up = lane >= 32;
#pragma unroll
    for (int mp = 0; mp < 2; ++mp) {
      float g[4], u[4];
#pragma unroll
      for (int rr = 0; rr < 4; ++rr) {
        auto sw = __builtin_amdgcn_permlane32_swap(__float_as_uint(acc[8][2 * mp][rr]), __float_as_uint(acc[8][2 * mp + 1][rr]), false, false);
        g[rr] = __uint_as_float(sw[0]);
        u[rr] = __uint_as_float(sw[1]);
      }
      const float rsv = up ? rs[2 * mp + 1] : rs[2 * mp], rnv = rsv * -1.4426950408889634f;
      const int m = 2 * mp + (up ? 1 : 0);
      const int gam = 2 * wn + gb;
      *(LAS u32x2*)(hb + 8 * 8192 + m * 1024 + (fr + 16 * (gam & 3)) * 16 + (gam >> 2) * 8) =
          (u32x2){pk2(glu(g[0], u[0], rsv, rnv), glu(g[1], u[1], rsv, rnv)), pk2(glu(g[2], u[2], rsv, rnv), glu(g[3], u[3], rsv, rnv))};
    }
  }
  wait_bar<4>();
  bf16x8 HA[9][4];
  {
    const lptr hb = L + F_H + (wm * 4) * 1024 + lane * 16;
    lptr hb2 = hb + 4 * 8192;
    asm volatile("" : "+v"(hb2));
#pragma unroll
    for (int k = 0; k < 9; ++k)
#pragma unroll
      for (int m = 0; m < 4; ++m) HA[k][m] = ldfrag(k < 4 ? hb + k * 8192 + m * 1024 : hb2 + (k - 4) * 8192 + m * 1024);
  }
  lptr pD[4];
  {
    const int rho = 4 * fq + q4;
    pD[0] = L + rho * 128 + ((wn ^ ((rho >> 1) & 3)) * 32) + 8 * pp;
    pD[1] = pD[0] + f_wslot(1); pD[2] = pD[0] + f_wslot(2); pD[3] = pD[0] + f_wslot(3);
    asm volatile("" : "+v"(pD[1]), "+v"(pD[2]), "+v"(pD[3]));
  }
  auto rdD = [&](int c, int k) __attribute__((always_inline)) { B[k] = cat8(trd(pD[c & 3] + k * 4096), trd(pD[c & 3] + k * 4096 + 2048)); };
#pragma unroll
  for (int k = 0; k < 8; ++k) rdD(0, k);
  lds_bar();
  issue2a(2); issue2b(2); issue2a(3); issue2b(3);
  STAMP(4);
  char* part = ws + WS_PART + ((((size_t)fs * 16 + mh) * 9 * 4 + wn) * 2 + wm) * 2048 + lane * 16;
  f32x4 o[2][4];
  auto st2 = [&](int c, int mp) __attribute__((always_inline)) {
    const f32x4 a = o[c & 1][2 * mp], b = o[c & 1][2 * mp + 1];
    const u32x4 v = {pk2(a[0], a[1]), pk2(a[2], a[3]), pk2(b[0], b[1]), pk2(b[2], b[3])};
    asm volatile("global_store_dwordx4 %0, %1, off sc1\n\ts_nop 1" ::"v"(part + (size_t)c * 16384 + mp * 1024), "v"(v) : "memory");
  };
  auto grd = [&](int c, int k) __attribute__((always_inline)) {
#pragma unroll
    for (int m = 0; m < 4; ++m) o[c & 1][m] = MFMA16(B[k], HA[k][m], k == 0 ? ((f32x4){0.f, 0.f, 0.f, 0.f}) : o[c & 1][m]);
  };
  SBAR();
#pragma unroll
  for (int c = 0; c < 9; ++c) {
    const bool nx = c < 8;
    grd(c, 0); rdD(c, 8); SBAR();
    grd(c, 1); if (c > 0) st2(c - 1, 0); SBAR();
    grd(c, 2); if (c > 0) st2(c - 1, 1); SBAR();
    if (nx) {
      if (c == 0) wait_bar<8>();
      else if (c == 1) wait_bar<10>();
      else if (c == 2) wait_bar<12>();
      else if (c <= 5) wait_bar<14>();
      else if (c == 6) wait_bar<10>();
      else wait_bar<6>();
    }
    grd(c, 3);
    if (c + 4 < 9) issue2a(c + 4);
    if (nx) rdD(c + 1, 0);
    SBAR();
    grd(c, 4);
    if (c + 4 < 9) issue2b(c + 4);
    if (nx) rdD(c + 1, 1);
    SBAR();
    grd(c, 5); if (nx) { rdD(c + 1, 2); rdD(c + 1, 3); } SBAR();
    grd(c, 6); if (nx) { rdD(c + 1, 4); rdD(c + 1, 5); } SBAR();
    grd(c, 7); if (nx) rdD(c + 1, 6); SBAR();
    grd(c, 8); if (nx) rdD(c + 1, 7); SBAR();
  }
  st2(8, 0); st2(8, 1);
  STAMP(5);
  STAMP(6); STAMP_FLUSH(3);
}

__global__ __launch_bounds__(256) void kr_reduce(const char* __restrict__ ws, u16* __restrict__ out) {
  STAMP_DECL; STAMP(0);
  const int lane = threadIdx.x & 63, unit = blockIdx.x * 4 + (threadIdx.x >> 6);
  const int fr = lane & 15, fq = lane >> 4;
  const int mp = unit & 1, wm = (unit >> 1) & 1, wn = (unit >> 2) & 3, cbm = unit >> 4, cb = cbm % 9, mh = cbm / 9;
  const char* p = ws + WS_PART + (size_t)unit * 1024 + lane * 16;
  u32x4 v[16];
#pragma unroll
  for (int f = 0; f < 16; ++f)
    v[f] = *(const u32x4*)(p + (size_t)f * 2304 * 1024);
  const int sub = cb * 4 + wn;
  const int ms0 = mh * 8 + wm * 4 + 2 * mp;
  u32x2 hc[2]; float mu[2];
#pragma unroll
  for (int i = 0; i < 2; ++i) {
    const int ms = ms0 + i;
    hc[i] = *(const u32x2*)(ws + WS_HCF + (((size_t)(ms >> 4) * 18 + (sub >> 1)) * 16 + (ms & 15)) * 1024 + lane * 16 + (sub & 1) * 8);
    mu[i] = ((const float*)(ws + WS_MEAN2))[ms * 16 + fr];
  }
  float s[8];
#pragma unroll
  for (int j = 0; j < 8; ++j) s[j] = 0.f;
#pragma unroll
  for (int f = 0; f < 16; ++f)
#pragma unroll
    for (int j = 0; j < 4; ++j) { s[2 * j] += bflo(v[f][j]); s[2 * j + 1] += bfhi(v[f][j]); }
#pragma unroll
  for (int i = 0; i < 2; ++i) {
    const u32x2 o = {pk2(s[4 * i] + (bflo(hc[i][0]) + mu[i]), s[4 * i + 1] + (bfhi(hc[i][0]) + mu[i])),
                     pk2(s[4 * i + 2] + (bflo(hc[i][1]) + mu[i]), s[4 * i + 3] + (bfhi(hc[i][1]) + mu[i]))};
    *(u32x2*)(out + (size_t)((ms0 + i) * 16 + fr) * 576 + sub * 16 + 4 * fq) = o;
  }
  STAMP(1);
  STAMP(2); STAMP_FLUSH(4);
}

extern "C" void kernel_launch(void* const* d_in, const int* in_sizes, int n_in, void* d_out, int out_size, void* d_ws, size_t ws_size,
                              hipStream_t stream) {
  const u16* x = (const u16*)d_in[0];
  const u16* wqkv = (const u16*)d_in[1];
  const u16* wo = (const u16*)d_in[2];
  const u16* wg = (const u16*)d_in[4];
  const u16* wu = (const u16*)d_in[5];
  const u16* wd = (const u16*)d_in[6];
  char* ws = (char*)d_ws;
  k1_qkv<<<256, 256, 0, stream>>>(x, wqkv, ws);
  k1b_fold<<<256, 256, 0, stream>>>(wo, ws);
  k2_wo<<<256, 512, 0, stream>>>(x, ws);
  kf_ffn<<<256, 512, 0, stream>>>(wg, wu, wd, ws);
  kr_reduce<<<576, 256, 0, stream>>>(ws, (u16*)d_out);
}
```

```cpp
#include <hip/hip_runtime.h>
#include <hip/hip_bf16.h>
#include <stdint.h>

typedef unsigned short u16;
typedef short bf16x8 __attribute__((ext_vector_type(8)));
typedef short s16x4 __attribute__((ext_vector_type(4)));
typedef float f32x4 __attribute__((ext_vector_type(4)));
typedef float f32x2 __attribute__((ext_vector_type(2)));
typedef unsigned u32x4 __attribute__((ext_vector_type(4)));
typedef unsigned u32x2 __attribute__((ext_vector_type(2)));
typedef __bf16 bf16x2_t __attribute__((ext_vector_type(2)));
typedef __amdgpu_buffer_rsrc_t rsrc_t;
#define LAS __attribute__((address_space(3)))
#define DI __device__ __forceinline__
#define AI __attribute__((always_inline))
typedef LAS char* lptr;

#define MFMA16(a, b, c) __builtin_amdgcn_mfma_f32_16x16x32_bf16((a), (b), (c), 0, 0, 0)
#define SBAR() __builtin_amdgcn_sched_barrier(0)
#define PIN(x) asm volatile("" : "+v"(x))
#define MFMA_ACC(c, a, b) asm("v_mfma_f32_16x16x32_bf16 %0, %1, %2, %0" : "+v"(c) : "v"(a), "v"(b))
#define MFMA_NEW(c, a, b) asm("v_mfma_f32_16x16x32_bf16 %0, %1, %2, 0" : "=&v"(c) : "v"(a), "v"(b))

constexpr int REC = 3328;
constexpr int WS_EPOCH = 0;
constexpr int WS_F1 = 256;
constexpr int WS_F2 = WS_F1 + 256 * 64;
constexpr int WS_F3 = WS_F2 + 256 * 64;
constexpr int WS_F4 = WS_F3 + 256 * 64;
constexpr int WS_REC = WS_F4 + 256 * 64;
constexpr int WS_MEAN1 = WS_REC + 256 * REC;
constexpr int WS_AF = WS_MEAN1 + 8192;
constexpr int WS_WEF = WS_AF + 2048 * 576 * 2;
constexpr int WS_RMF = WS_WEF + 8 * 18 * 4608;
constexpr int WS_HCF = WS_RMF + 8 * 2048;
constexpr int WS_SSQ = WS_HCF + 2048 * 576 * 2;
constexpr int WS_MEAN2 = WS_SSQ + 16 * 8192;
constexpr int WS_PART = WS_MEAN2 + 8192;
constexpr int WS_END = WS_PART + 16 * 2048 * 576 * 2;

template <int I> struct IC { static constexpr int v = I; };
template <int I, int N, class Fn> DI void static_for(Fn&& f) {
  if constexpr (I < N) { f(IC<I>{}); static_for<I + 1, N>(f); }
}
DI unsigned pk2(float a, float b) {
  bf16x2_t v = {(__bf16)a, (__bf16)b};
  return __builtin_bit_cast(unsigned, v);
}
DI float bflo(unsigned w) { return __uint_as_float(w << 16); }
DI float bfhi(unsigned w) { return __uint_as_float(w & 0xffff0000u); }
DI s16x4 trd(lptr p) { return __builtin_bit_cast(s16x4, __builtin_amdgcn_ds_read_tr16_b64_v4i16((LAS s16x4*)p)); }
DI bf16x8 cat8(s16x4 lo, s16x4 hi) { return __builtin_shufflevector(lo, hi, 0, 1, 2, 3, 4, 5, 6, 7); }
DI bf16x8 ldfrag(lptr p) { return *(LAS bf16x8*)p; }
DI rsrc_t mkrs(const void* p, int bytes) { return __builtin_amdgcn_make_buffer_rsrc((void*)p, (short)0, bytes, 0x00020000); }
template <int LOFF, int GOFF> DI void dma(rsrc_t rs, unsigned voff, unsigned wl) {
  unsigned t;
  asm volatile("s_add_u32 m0, %3, %4\n\ts_mov_b32 %0, %5\n\tbuffer_load_dwordx4 %1, %2, %0 offen lds"
               : "=&s"(t) : "v"(voff), "s"(rs), "s"(wl), "i"(LOFF), "i"(GOFF) : "memory", "scc");
}
template <int LOFF, int GOFF> DI void dma_dev(rsrc_t rs, unsigned voff, unsigned wl) {
  unsigned t;
  asm volatile("s_add_u32 m0, %3, %4\n\ts_mov_b32 %0, %5\n\tbuffer_load_dwordx4 %1, %2, %0 offen sc1 lds"
               : "=&s"(t) : "v"(voff), "s"(rs), "s"(wl), "i"(LOFF), "i"(GOFF) : "memory", "scc");
}
template <int LOFF> DI void touch(rsrc_t rs, unsigned voff, unsigned wl) {
  asm volatile("s_add_u32 m0, %2, %3\n\ts_nop 0\n\tbuffer_load_dword %0, %1, 0 offen lds" ::"v"(voff), "s"(rs), "s"(wl), "i"(LOFF) : "memory", "scc");
}
DI void st16_dev(rsrc_t rs, unsigned voff, u32x4 v) { __builtin_amdgcn_raw_buffer_store_b128(v, rs, voff, 0, 16); }
DI void st8_dev(rsrc_t rs, unsigned voff, u32x2 v) { __builtin_amdgcn_raw_buffer_store_b64(v, rs, voff, 0, 16); }
DI void st4_dev(rsrc_t rs, unsigned voff, unsigned v) { __builtin_amdgcn_raw_buffer_store_b32(v, rs, voff, 0, 16); }
DI u32x4 ld16_dev(rsrc_t rs, unsigned voff, int soff) { return __builtin_amdgcn_raw_buffer_load_b128(rs, voff, soff, 16); }
DI u32x2 ld8_dev(rsrc_t rs, unsigned voff) { return __builtin_amdgcn_raw_buffer_load_b64(rs, voff, 0, 16); }
DI unsigned ld4_dev(rsrc_t rs, unsigned voff) { return __builtin_amdgcn_raw_buffer_load_b32(rs, voff, 0, 16); }
template <int N> DI void wait_bar() { asm volatile("s_waitcnt vmcnt(%0) lgkmcnt(0)\n\ts_barrier" ::"i"(N) : "memory"); }
DI void lds_bar() { asm volatile("s_waitcnt lgkmcnt(0)\n\ts_barrier" ::: "memory"); }
DI float phi_f(float t) { return (t > 0.f ? t + 1.f : __builtin_amdgcn_exp2f(t * 1.4426950408889634f)) + 1e-6f; }
DI float xor16(float v) { auto r = __builtin_amdgcn_permlane16_swap(__float_as_uint(v), __float_as_uint(v), false, false); return __uint_as_float(r[0]) + __uint_as_float(r[1]); }
DI float xor32(float v) { auto r = __builtin_amdgcn_permlane32_swap(__float_as_uint(v), __float_as_uint(v), false, false); return __uint_as_float(r[0]) + __uint_as_float(r[1]); }
DI float sum_fq(float v) { return xor32(xor16(v)); }
DI void poll(rsrc_t rs, unsigned voff, unsigned t0, unsigned t1, unsigned need) {
#pragma unroll 1
  for (unsigned spin = 0; spin < (1u << 17); ++spin) {
    u32x2 f;
    asm volatile("buffer_load_dwordx2 %0, %1, %2, 0 offen sc1\n\ts_waitcnt vmcnt(0)" : "=v"(f) : "v"(voff), "s"(rs) : "memory");
    const unsigned d = f[0] - t0;
    if (__all(f[1] == t1 && d >= need && d < 16u)) break;
    __builtin_amdgcn_s_sleep(1);
  }
}

#define STAMP_DECL
#define STAMP(i) do {} while (0)

constexpr int KA_XR = 0;
constexpr int KA_KV = KA_XR + 3 * 16384;
constexpr int KA_Q = KA_KV + 3 * 10240;
constexpr int KA_EX = KA_Q + 576 * 80;
constexpr int KA_WO = KA_EX + 128 * 160;
constexpr int KA_KVI = KA_WO + 3072;
constexpr int KA_RSP = KA_KVI + 6144;
constexpr int KA_ZZ = KA_RSP + 160;
constexpr int KA_RS = KA_ZZ + 160;
constexpr int KA_DEAD = KA_RS + 320;
static_assert(KA_DEAD + 2048 <= 163840, "lds");

struct Tab { unsigned v[1024]; };
constexpr Tab make_tab() {
  Tab T{};
  for (int t = 0; t < 512; ++t) {
    const int w = t >> 6, ln = t & 63, fr = ln & 15, fq = ln >> 4;
    const int slot = w == 0 ? ln : w == 3 ? 64 + ln : w == 1 ? (fr < 2 ? 128 + fq * 2 + fr : -1) : w == 2 ? (fq == 0 ? 136 + fr : -1)
                   : w == 4 ? (fr == 8 ? 152 + fq : (ln < 8 ? 197 + ln : ln == 9 ? 205 : -1)) : w == 5 ? ((fr >= 4 && fr <= 8) ? 156 + fq * 5 + fr - 4 : -1)
                   : w == 6 ? (fq == 0 ? 176 + fr : -1) : ((fq == 0 && fr >= 4 && fr <= 8) ? 192 + fr - 4 : -1);
    int ce = 0, cd = 0;
    if (t < 128) { ce = (t & 15) + (t >> 6) * 16; cd = ((t >> 4) & 3) * 4 + (t >> 6) * 16; }
    else if (t < 136) { ce = 16 + (t & 1); cd = ((t - 128) >> 1) * 4; }
    else if (t < 152) { ce = t - 136; cd = 16; }
    else if (t < 156) { ce = 36; cd = (t - 152) * 4; }
    else if (t < 176) { const int i = t - 156, f = i / 5; ce = 32 + i - f * 5; cd = 16 + 4 * f; }
    else if (t < 192) { ce = t - 160; cd = 32; }
    else if (t < 197) { ce = 32 + t - 192; cd = 32; }
    int m = 0;
    for (int rr = 0; rr < 4; ++rr) m |= ((cd + rr >= 18) == (ce >= 18)) ? (1 << rr) : 0;
    int dst = KA_KVI + ce * 96 + cd * 2;
    if (ce == 36) { dst = KA_ZZ + cd * 4; m = 16; }
    if (t >= 197) { dst = KA_RS + (t < 206 ? (t - 197) * 32 : 0); m = t < 206 ? 32 : 64; }
    T.v[2 * t] = slot >= 0 ? (unsigned)slot * 16u : 0x40000000u;
    T.v[2 * t + 1] = (unsigned)dst | ((unsigned)m << 24);
  }
  return T;
}
static __device__ const Tab g_tab = make_tab();

constexpr int KB_B = 18 * 4096;
constexpr int KB_RM = KB_B + 18 * 4608;
constexpr int F_G = 8192, F_U = 8192 + 18432 + 16, F_STG = F_U + 18432;
constexpr int F_SSQ = 3 * F_STG;
constexpr int F_RS = F_SSQ + 8192;
constexpr int F_H = 163840 - 73728;
constexpr int f_wslot(int i) { return i == 0 ? 0 : i == 1 ? F_STG : i == 2 ? F_H : F_H + 36864; }

__global__ __launch_bounds__(512, 2) void mk_block(const u16* __restrict__ x, const u16* __restrict__ wqkv, const u16* __restrict__ wo,
                                                   const u16* __restrict__ wg, const u16* __restrict__ wu, const u16* __restrict__ wd,
                                                   char* __restrict__ ws, u16* __restrict__ out) {
  __shared__ __attribute__((aligned(16))) char smem[163840];
  STAMP_DECL; STAMP(0);
  const lptr L = (lptr)smem;
  const unsigned lds0 = (unsigned)(uintptr_t)L;
  const int tid = threadIdx.x, lane = tid & 63, w = __builtin_amdgcn_readfirstlane(tid >> 6);
  const int fr = lane & 15, fq = lane >> 4, q4 = fr >> 2, pp = lane & 3;
  const int xcd = blockIdx.x & 7, r = blockIdx.x >> 3;
  const unsigned wl1 = lds0 + w * 1024, wl2 = lds0 + w * 2048;
  const rsrc_t rW = mkrs(ws, WS_END), rX = mkrs(x, 2048 * 576 * 2);
  const rsrc_t rG = mkrs(wg, 576 * 4608 * 2), rU = mkrs(wu, 576 * 4608 * 2);
  const f32x4 z4 = {0.f, 0.f, 0.f, 0.f};
  unsigned tag0, tag1;

  {
    const rsrc_t rQ = mkrs(wqkv, 576 * 1728 * 2);
    const int tt = (xcd & 3) * 4 + (r & 3), p = (xcd >> 2) * 8 + (r >> 2);
    const int o8 = (p & 1) * 8, ot = (tt & 1) * 8;
    const int tok0 = tt * 128 + w * 16;
    const unsigned vx = (unsigned)((tok0 + (lane >> 3)) * 1152 + (((lane & 7) ^ (lane >> 3)) * 16));
    dma<KA_XR, 0>(rX, vx, wl2);
    dma<KA_XR + 1024, 8 * 1152>(rX, vx, wl2);
    auto mkv = [&](int pi) AI {
      const int c = pi * 64 + lane, row = c / 10, cc = c - row * 10, rho = row & 31;
      const int k = (row & 32) + ((rho >> 2) & 3) * 8 + (rho >> 4) * 4 + (rho & 3);
      return (unsigned)(k * 3456 + (cc >= 5 ? 1152 - 80 : 0) + cc * 16 + 1152 + 72 * p - o8);
    };
    const unsigned vkv0 = mkv(w), vkv8 = mkv(8 + (w & 1));
    const bool xtra = w < 2;
    auto piece = [&](auto kbc, int i) AI {
      constexpr int kb = decltype(kbc)::v, sl = kb % 3;
      if (i == 0) dma<KA_XR + sl * 16384, kb * 128>(rX, vx, wl2);
      if (i == 1) dma<KA_XR + sl * 16384 + 1024, kb * 128 + 8 * 1152>(rX, vx, wl2);
      if (i == 2) dma<KA_KV + sl * 10240, kb * 64 * 3456>(rQ, vkv0, wl1);
      if (i == 3) { if (xtra) dma<KA_KV + sl * 10240 + 8192, kb * 64 * 3456>(rQ, vkv8, wl1); }
    };
    piece(IC<0>{}, 2); piece(IC<0>{}, 3);
    piece(IC<1>{}, 0); piece(IC<1>{}, 1); piece(IC<1>{}, 2); piece(IC<1>{}, 3);
    u32x2 ep, tab;
    asm volatile("buffer_load_dwordx2 %0, off, %1, 0 sc1" : "=v"(ep) : "s"(rW) : "memory");
    asm volatile("global_load_dwordx2 %0, %1, %2" : "=v"(tab) : "v"(tid * 8), "s"(g_tab.v) : "memory");
    if (w >= 5) {
      const int c = min((w - 5) * 64 + lane, 179), rw = c / 5;
      dma<KA_WO - 5 * 1024, 0>(mkrs(wo, 576 * 576 * 2), (unsigned)((36 * p + rw) * 1152 + 72 * tt - ot + (c - rw * 5) * 16), wl1);
    }
    piece(IC<2>{}, 0); piece(IC<2>{}, 1); piece(IC<2>{}, 2); piece(IC<2>{}, 3);
    if (tid < 384) *(LAS u32x4*)(L + KA_KVI + tid * 16) = (u32x4){0u, 0u, 0u, 0u};
    unsigned vq;
    {
      const int c = (w < 5 ? w : 0) * 64 + lane, row = c / 5, ch = c - row * 5;
      const int rho = row & 31, rr = rho & 15;
      vq = (unsigned)(((row & 32) + 8 * (2 * (rr & 1) + (rr >> 3)) + 4 * (rho >> 4) + ((rr >> 1) & 3)) * 3456 + ch * 16 + 72 * p - o8);
    }
    unsigned vt0, vt1;
    {
      const int l0 = min((r * 8 + w) * 22 + min(lane, 10), 5183), l1 = min(l0 + 11, 5183), r0 = l0 / 9, r1 = l1 / 9;
      vt0 = (unsigned)(r0 * 9216 + (l0 - r0 * 9) * 128 + xcd * 1152);
      vt1 = (unsigned)(r1 * 9216 + (l1 - r1 * 9) * 128 + xcd * 1152);
    }
    const lptr bk = L + KA_KV + (4 * fq + q4) * 160 + o8 + 8 * pp;
    const lptr bx = L + KA_KV + (4 * fq + q4) * 160 + 80 * (pp & 1) + o8 + 64;
    const lptr ba0 = L + KA_XR + w * 2048 + fr * 128 + (fq ^ (fr & 7)) * 16;
    const lptr ba1 = L + KA_XR + w * 2048 + fr * 128 + ((4 + fq) ^ (fr & 7)) * 16;
    f32x4 acc[5], cs[5], gram = z4, sx = z4;
#pragma unroll
    for (int i = 0; i < 5; ++i) acc[i] = cs[i] = z4;
    bf16x8 ones = {0x3F80, 0x3F80, 0x3F80, 0x3F80, 0x3F80, 0x3F80, 0x3F80, 0x3F80};
    PIN(ones);
    bf16x8 a[18], B[2][5];
    auto readA = [&](int kb) AI {
      a[2 * kb] = ldfrag(ba0 + (kb % 3) * 16384);
      a[2 * kb + 1] = ldfrag(ba1 + (kb % 3) * 16384);
    };
    auto readB = [&](int s, bf16x8(&b)[5]) AI {
      const int so = ((s >> 1) % 3) * 10240 + (s & 1) * 5120;
#pragma unroll
      for (int i = 0; i < 4; ++i) {
        const int off = so + (i >> 1) * 80 + 32 * (i & 1);
        b[i] = cat8(trd(bk + off), trd(bk + off + 2560));
      }
      b[4] = cat8(trd(bx + so), trd(bx + so + 2560));
    };
    auto mma = [&](int s, const bf16x8(&b)[5], int part) AI {
#pragma unroll
      for (int i = 2 * part; i < 2 * part + 2 && i < 5; ++i) {
        acc[i] = MFMA16(b[i], a[s], acc[i]);
        cs[i] = MFMA16(b[i], ones, cs[i]);
      }
      if (part == 2) { gram = MFMA16(a[s], a[s], gram); sx = MFMA16(ones, a[s], sx); }
      SBAR();
    };
    auto issue_q3 = [&](auto kc) AI {
      constexpr int k = decltype(kc)::v;
      if (w < 5) {
        dma<KA_Q + k * 5120, k * 64 * 3456>(rQ, vq, wl1);
        dma<KA_Q + (k + 1) * 5120, (k + 1) * 64 * 3456>(rQ, vq, wl1);
        dma<KA_Q + (k + 2) * 5120, (k + 2) * 64 * 3456>(rQ, vq, wl1);
      }
    };
    STAMP(1);
    wait_bar<8>();
    STAMP(2);
    readA(0);
    readB(0, B[0]);
    static_for<0, 9>([&](auto ic) AI {
      constexpr int KB = decltype(ic)::v;
      readB(2 * KB + 1, B[1]);
      SBAR();
      mma(2 * KB, B[0], 0);
      if constexpr (KB >= 1 && KB <= 6) { piece(IC<KB + 2>{}, 3); SBAR(); }
      if constexpr (KB == 7) { issue_q3(IC<3>{}); SBAR(); }
      mma(2 * KB, B[0], 1);
      mma(2 * KB, B[0], 2);
      if constexpr (KB < 8) {
        if constexpr (KB == 0) wait_bar<5>();
        else if constexpr (KB <= 2) wait_bar<3>();
        else if constexpr (KB == 3) wait_bar<4>();
        else if constexpr (KB <= 6) wait_bar<5>();
        else { if (w < 5) wait_bar<6>(); else wait_bar<0>(); }
        readA(KB + 1);
        readB(2 * KB + 2, B[0]);
        SBAR();
      }
      mma(2 * KB + 1, B[1], 0);
      if constexpr (KB <= 5) { piece(IC<KB + 3>{}, 0); SBAR(); }
      if constexpr (KB == 6) { issue_q3(IC<0>{}); SBAR(); }
      if constexpr (KB == 7) { issue_q3(IC<6>{}); SBAR(); }
      mma(2 * KB + 1, B[1], 1);
      if constexpr (KB <= 5) { piece(IC<KB + 3>{}, 1); SBAR(); }
      if constexpr (KB == 3) {
        if (tid < 36) {
          float s = 0.f;
#pragma unroll
          for (int i = 0; i < 9; ++i) {
            const u32x2 v = *(LAS u32x2*)(L + KA_WO + tid * 80 + ot + i * 8);
            s += (bflo(v[0]) + bfhi(v[0])) + (bflo(v[1]) + bfhi(v[1]));
          }
          *(LAS float*)(L + KA_RSP + tid * 4) = s;
        }
        SBAR();
      }
      mma(2 * KB + 1, B[1], 2);
      if constexpr (KB <= 5) { piece(IC<KB + 3>{}, 2); SBAR(); }
      if constexpr (KB >= 2 && KB <= 5) { touch<KA_DEAD>((KB & 1) ? rU : rG, KB < 4 ? vt0 : vt1, lds0 + w * 256); SBAR(); }
    });
    STAMP(3);
    float meanT, rstdT;
    {
      const int rr = fr & 3;
      float g = rr == 0 ? gram[0] : rr == 1 ? gram[1] : rr == 2 ? gram[2] : gram[3];
      g = sum_fq(q4 == fq ? g : 0.f);
      meanT = sx[0] * (1.f / 576.f);
      rstdT = __builtin_amdgcn_rsqf(fmaxf(g * (1.f / 576.f) - meanT * meanT, 0.f) + 1e-5f);
      const lptr exw = L + KA_EX + (w * 16 + fr) * 160 + fq * 8;
#pragma unroll
      for (int i = 0; i < 5; ++i) {
        float v[4];
#pragma unroll
        for (int k = 0; k < 4; ++k) {
          const float t = (acc[i][k] - meanT * cs[i][k]) * rstdT;
          if (i < 2) v[k] = phi_f(t);
          else if (i < 4) v[k] = t;
          else { const float ph = phi_f(t); v[k] = fq == 0 ? ph : (fq == 1 ? t : 1.0f); }
        }
        *(LAS u32x2*)(exw + i * 32) = (u32x2){pk2(v[0], v[1]), pk2(v[2], v[3])};
      }
    }
    STAMP(4);
    asm volatile("s_waitcnt vmcnt(0) lgkmcnt(0)\n\ts_barrier" : "+v"(ep), "+v"(tab)::"memory");
    const unsigned recb = WS_REC + p * 16 * REC;
    {
      const int sa = (0x44101100 >> (4 * w)) & 7, sb = (0x43443232 >> (4 * w)) & 7;
      const lptr ex = L + KA_EX + (4 * fq + q4) * 160 + pp * 8;
      const lptr exa = ex + sa * 32, exb = ex + sb * 32;
      bf16x8 fa[4], fb[4];
#pragma unroll
      for (int k = 0; k < 4; ++k) {
        fa[k] = cat8(trd(exa + k * 5120), trd(exa + k * 5120 + 2560));
        fb[k] = cat8(trd(exb + k * 5120), trd(exb + k * 5120 + 2560));
      }
      const f32x4 rsp = *(LAS f32x4*)(L + KA_RSP + min(lane, 8) * 16);
      f32x4 T = z4;
#pragma unroll
      for (int k = 0; k < 4; ++k) T = MFMA16(fa[k], fb[k], T);
      if (tab[0] >= 197 * 16) T = rsp;
      st16_dev(rW, recb + tt * REC + tab[0], __builtin_bit_cast(u32x4, T));
    }
    STAMP(5);
    tag0 = __builtin_amdgcn_readfirstlane((ep[0] * 0x9E3779B1u + 0x7F4A7C15u + ep[1]) & ~15u);
    tag1 = __builtin_amdgcn_readfirstlane((ep[1] ^ (ep[0] >> 7)) * 0x85EBCA6Bu + 0x165667B1u);
    if (tag1 == (unsigned)__builtin_amdgcn_readfirstlane(ep[1])) tag1 ^= 0x5bd1e995u;
    f32x4 qa[3];
    {
      f32x4 qc[3];
#pragma unroll
      for (int i = 0; i < 3; ++i) qa[i] = qc[i] = z4;
      const lptr bq = L + KA_Q + (2 * (4 * (fq & 1) + q4) + (fq >> 1)) * 80 + o8;
      const lptr bqp = bq + 8 * pp;
      bf16x8 b[3][3];
      auto rq = [&](int s) AI {
        b[s % 3][0] = cat8(trd(bqp + s * 2560), trd(bqp + s * 2560 + 1280));
        b[s % 3][1] = cat8(trd(bqp + s * 2560 + 32), trd(bqp + s * 2560 + 1280 + 32));
        b[s % 3][2] = cat8(trd(bq + s * 2560 + 64), trd(bq + s * 2560 + 1280 + 64));
      };
      rq(0); rq(1);
      static_for<0, 18>([&](auto ic) AI {
        constexpr int s = decltype(ic)::v;
        if constexpr (s < 16) rq(s + 2);
#pragma unroll
        for (int i = 0; i < 3; ++i) qa[i] = MFMA16(b[s % 3][i], a[s], qa[i]);
        if ((w & 1) == (s & 1)) {
#pragma unroll
          for (int i = 0; i < 3; ++i) qc[i] = MFMA16(b[s % 3][i], ones, qc[i]);
        }
        SBAR();
        if constexpr (s == 7) {
          asm volatile("s_waitcnt vmcnt(0)\n\ts_barrier" ::: "memory");
          if (tid == 0) st8_dev(rW, WS_F1 + (p * 16 + tt) * 64, (u32x2){tag0, tag1});
          STAMP(6);
        }
      });
      if (fr == 0) {
#pragma unroll
        for (int i = 0; i < 3; ++i) *(LAS f32x4*)(L + KA_XR + w * 256 + (i * 4 + fq) * 16) = qc[i];
      }
      if (p == 0 && fq == 0) st4_dev(rW, WS_MEAN1 + (tok0 + fr) * 4, __float_as_uint(meanT));
      STAMP(7);
      u32x4 v[16];
      if (w < 4) {
        poll(rW, WS_F1 + (p * 16 + fr) * 64, tag0, tag1, 0);
        STAMP(8);
        const unsigned vo = recb + min(tid, 205) * 16;
#pragma unroll
        for (int t = 0; t < 16; ++t) v[t] = ld16_dev(rW, vo, t * REC);
      }
      lds_bar();
      float pq[3][4];
#pragma unroll
      for (int i = 0; i < 3; ++i) {
        const f32x4 cq = qc[i] + *(LAS f32x4*)(L + KA_XR + (w ^ 1) * 256 + (i * 4 + fq) * 16);
#pragma unroll
        for (int k = 0; k < 4; ++k) pq[i][k] = phi_f((qa[i][k] - meanT * cq[k]) * rstdT);
      }
      SBAR();
      STAMP(9);
      if (w < 4) {
#define BC(i) __builtin_bit_cast(f32x4, v[i])
        const f32x4 s = (((BC(0) + BC(1)) + (BC(2) + BC(3))) + ((BC(4) + BC(5)) + (BC(6) + BC(7)))) +
                        (((BC(8) + BC(9)) + (BC(10) + BC(11))) + ((BC(12) + BC(13)) + (BC(14) + BC(15))));
#undef BC
        const unsigned cm = tab[1] >> 24;
        const lptr cd = L + (tab[1] & 0xffffff);
        if (cm < 16) {
          *(LAS u32x2*)cd = (u32x2){pk2((cm & 1) ? s[0] : 0.f, (cm & 2) ? s[1] : 0.f), pk2((cm & 4) ? s[2] : 0.f, (cm & 8) ? s[3] : 0.f)};
        } else if (cm == 16) {
          *(LAS f32x4*)cd = s;
        } else if (cm == 32) {
          const f32x4 t = s * (1.f / 576.f);
          *(LAS u32x4*)cd = (u32x4){pk2(t[0], 0.f), 0u, pk2(t[1], 0.f), 0u};
          *(LAS u32x4*)(cd + 16) = (u32x4){pk2(t[2], 0.f), 0u, pk2(t[3], 0.f), 0u};
        }
      }
      const int ct = w < 3 ? w : w < 6 ? w - 3 : 0;
      const lptr wa = L + KA_KVI + (4 * fq + q4) * 96 + (w < 3 ? 0 : 64) + pp * 8;
      lptr wb[4];
#pragma unroll
      for (int tq = 0; tq < 4; ++tq) {
        const int ec = tq < 2 ? 16 * tq + 4 * fq + q4 : min(16 * tq + 4 * fq + q4, 35);
        wb[tq] = (ct == 2 && pp > 0) ? L + KA_RS + ec * 8 : L + KA_WO + ec * 80 + ot + ct * 32 + pp * 8;
      }
      unsigned wst;
      {
        const int c36 = 16 * ct + fr, c72 = (tt & 1) * 36 + c36, sub = c72 >> 4, frc = c72 & 15;
        const int ks = w < 3 ? p : 16 + (p >> 3), fqq = w < 3 ? fq : (p & 3), hb = w < 3 ? 0 : ((p >> 2) & 1) * 8;
        wst = WS_WEF + ((tt >> 1) * 18 + ks) * 4608 + (sub < 4 ? sub * 1024 + (frc + 16 * fqq) * 16 : 4096 + (frc + 8 * fqq) * 16) + hb;
        if (c36 == 36) wst = WS_RMF + (tt >> 1) * 2048 + (ks * 4 + fqq) * 16 + hb;
        if (c36 > 36 || (c36 == 36 && (tt & 1)) || (w >= 3 && fq != 0) || w >= 6) wst = 0x40000000u;
      }
      const unsigned afb = WS_AF + (tok0 >> 6) * 18 * 4096 + ((tok0 >> 4) & 3) * 1024;
      lds_bar();
      STAMP(10);
      {
        bf16x8 fa[2][2], fb[2];
#pragma unroll
        for (int k = 0; k < 2; ++k) {
          fa[0][k] = cat8(trd(wa + k * 3072), trd(wa + k * 3072 + 1536));
          fa[1][k] = cat8(trd(wa + k * 3072 + 32), trd(wa + k * 3072 + 1536 + 32));
          fb[k] = cat8(trd(wb[2 * k]), trd(wb[2 * k + 1]));
        }
        f32x4 zz[3];
#pragma unroll
        for (int i = 0; i < 3; ++i) zz[i] = *(LAS f32x4*)(L + KA_ZZ + (i < 2 ? 16 * i + 4 * fq : 32) * 4);
        const f32x4 T0 = MFMA16(fa[0][1], fb[1], MFMA16(fa[0][0], fb[0], z4));
        const f32x4 T1 = MFMA16(fa[1][1], fb[1], MFMA16(fa[1][0], fb[0], z4));
        float d0 = (pq[0][0] * zz[0][0] + pq[0][1] * zz[0][1]) + (pq[0][2] * zz[0][2] + pq[0][3] * zz[0][3]);
        const float lo2 = pq[1][0] * zz[1][0] + pq[1][1] * zz[1][1];
        float d1 = pq[1][2] * zz[1][2] + pq[1][3] * zz[1][3];
        const float x2 = (pq[2][0] * zz[2][0] + pq[2][1] * zz[2][1]) + (pq[2][2] * zz[2][2] + pq[2][3] * zz[2][3]);
        d0 += fq == 0 ? lo2 : 0.f;
        d1 += fq == 0 ? x2 : lo2;
        d0 = sum_fq(d0);
        d1 = sum_fq(d1);
        const float i0 = __builtin_amdgcn_rcpf(d0 + 1e-6f), i1 = __builtin_amdgcn_rcpf(d1 + 1e-6f), il = fq == 0 ? i0 : i1;
        st16_dev(rW, afb + p * 4096 + lane * 16,
                 (u32x4){pk2(pq[0][0] * i0, pq[0][1] * i0), pk2(pq[0][2] * i0, pq[0][3] * i0), pk2(pq[1][0] * il, pq[1][1] * il), pk2(pq[1][2] * i1, pq[1][3] * i1)});
        if (fq == 0)
          st8_dev(rW, afb + (16 + (p >> 3)) * 4096 + (fr + 16 * (p & 3)) * 16 + ((p >> 2) & 1) * 8,
                  (u32x2){pk2(pq[2][0] * i1, pq[2][1] * i1), pk2(pq[2][2] * i1, pq[2][3] * i1)});
        const u32x4 wv = {pk2(T0[0], T0[1]), pk2(T0[2], T0[3]), pk2(T1[0], T1[1]), pk2(T1[2], T1[3])};
        if (w < 3) st16_dev(rW, wst, wv); else st8_dev(rW, wst, (u32x2){wv[0], wv[1]});
      }
    }
    STAMP(11);
    asm volatile("s_waitcnt vmcnt(0) lgkmcnt(0)\n\ts_barrier" ::: "memory");
    if (tid == 0) st8_dev(rW, WS_F2 + (p * 16 + tt) * 64, (u32x2){tag0, tag1});
    STAMP(12);
  }

  {
    const int t64 = (xcd & 3) * 8 + (r & 7), j = (xcd >> 2) * 4 + (r >> 3);
    const int msub = w & 3, kh = w >> 2;
    const int tok = t64 * 64 + msub * 16 + fr;
    u32x2 xv[3];
    {
      const char* xr = (const char*)x + (size_t)tok * 1152 + 144 * j;
      xv[0] = *(const u32x2*)(xr + kh * 64 + 8 * fq);
      xv[1] = *(const u32x2*)(xr + kh * 64 + 32 + 8 * fq);
      xv[2] = *(const u32x2*)(xr + 128 + 8 * (fq & 1));
    }
    if (w == 0) {
      const int i = lane < 48 ? lane : 0;
      poll(rW, WS_F2 + (i < 16 ? i * 16 + (t64 >> 1) : ((i - 16) >> 1) * 16 + 2 * j + (i & 1)) * 64, tag0, tag1, 0);
      if (blockIdx.x == 0 && lane == 0) st8_dev(rW, WS_EPOCH, (u32x2){tag0, tag1});
    }
    asm volatile("s_barrier" ::: "memory");
    STAMP(13);
    const unsigned va = WS_AF + t64 * 18 * 4096 + tid * 16, vb = WS_WEF + j * 18 * 4608 + tid * 16;
    if (w == 3 || w == 4) dma_dev<KB_RM - 3 * 1024, 0>(rW, WS_RMF + j * 2048 + tid * 16 - 3 * 1024, wl1);
    const unsigned m1 = ld4_dev(rW, WS_MEAN1 + tok * 4);
    static_for<0, 3>([&](auto gc) AI {
      constexpr int g = decltype(gc)::v;
      dma_dev<g * 24576, g * 24576>(rW, va, wl1);
      dma_dev<KB_B + g * 27648, g * 27648>(rW, vb, wl1);
      dma_dev<g * 24576 + 8192, g * 24576 + 8192>(rW, va, wl1);
      dma_dev<KB_B + g * 27648 + 8192, g * 27648 + 8192>(rW, vb, wl1);
      dma_dev<g * 24576 + 16384, g * 24576 + 16384>(rW, va, wl1);
      dma_dev<KB_B + g * 27648 + 16384, g * 27648 + 16384>(rW, vb, wl1);
      if (w < 3) dma_dev<KB_B + g * 27648 + 24576, g * 27648 + 24576>(rW, vb, wl1);
    });
    SBAR();
    f32x4 acc[5];
#pragma unroll
    for (int i = 0; i < 5; ++i) acc[i] = z4;
    const lptr pa = L + kh * 4096 + msub * 1024 + lane * 16;
    lptr pb[2];
    pb[0] = L + KB_B + kh * 4608 + lane * 16;
    pb[1] = pb[0] + 5 * 9216;
    PIN(pb[1]);
    lptr p4 = fr < 8 ? L + KB_B + kh * 4608 + 4096 + (fr + 8 * fq) * 16 : L + KB_RM + (kh * 4 + fq) * 16;
    const int p4s = fr < 8 ? 9216 : 128;
    static_for<0, 3>([&](auto gc) AI {
      constexpr int g = decltype(gc)::v;
      wait_bar<6 * (2 - g)>();
      if constexpr (g == 0) STAMP(14);
      bf16x8 av[3], bv[3][5];
#pragma unroll
      for (int k = 0; k < 3; ++k) {
        const int kb = 3 * g + k;
        av[k] = ldfrag(pa + kb * 8192);
#pragma unroll
        for (int i = 0; i < 4; ++i) bv[k][i] = ldfrag((kb < 5 ? pb[0] + kb * 9216 : pb[1] + (kb - 5) * 9216) + i * 1024);
        bv[k][4] = ldfrag(p4);
        p4 += p4s;
      }
#pragma unroll
      for (int k = 0; k < 3; ++k)
#pragma unroll
        for (int i = 0; i < 5; ++i) acc[i] = MFMA16(bv[k][i], av[k], acc[i]);
    });
    STAMP(15);
    lds_bar();
    {
      const lptr xs = L + (msub * 2 + kh) * 3072 + lane * 16;
      *(LAS f32x4*)(xs) = kh == 0 ? acc[2] : acc[0];
      *(LAS f32x4*)(xs + 1024) = kh == 0 ? acc[3] : acc[1];
      *(LAS f32x4*)(xs + 2048) = acc[4];
    }
    lds_bar();
    f32x4 o[3];
    {
      const lptr xs = L + (msub * 2 + (1 - kh)) * 3072 + lane * 16;
      o[0] = (kh == 0 ? acc[0] : acc[2]) + *(LAS f32x4*)(xs);
      o[1] = (kh == 0 ? acc[1] : acc[3]) + *(LAS f32x4*)(xs + 1024);
      o[2] = acc[4] + *(LAS f32x4*)(xs + 2048);
    }
    const float mean2 = __uint_as_float(m1) + sum_fq(fq == 2 ? o[2][0] : 0.f);
    u32x2 pk[3];
    float ssq = 0.f;
#pragma unroll
    for (int i = 0; i < 3; ++i) {
      pk[i] = (u32x2){pk2(bflo(xv[i][0]) + o[i][0] - mean2, bfhi(xv[i][0]) + o[i][1] - mean2),
                      pk2(bflo(xv[i][1]) + o[i][2] - mean2, bfhi(xv[i][1]) + o[i][3] - mean2)};
      const float a0 = bflo(pk[i][0]), a1 = bfhi(pk[i][0]), a2 = bflo(pk[i][1]), a3 = bfhi(pk[i][1]);
      const float q = (a0 * a0 + a1 * a1) + (a2 * a2 + a3 * a3);
      ssq += (i < 2 || (kh == 0 && fq < 2)) ? q : 0.f;
    }
    ssq = sum_fq(ssq);
    const unsigned hb = WS_HCF + ((tok >> 7) * 18 * 8 + ((tok >> 4) & 7)) * 1024;
    st16_dev(rW, hb + (2 * j + kh) * 8192 + lane * 16, (u32x4){pk[0][0], pk[0][1], pk[1][0], pk[1][1]});
    if (kh == 0 && fq < 2) st8_dev(rW, hb + (16 + (j >> 2)) * 8192 + (fr + 16 * (j & 3)) * 16 + fq * 8, pk[2]);
    if (fq == 0) st4_dev(rW, WS_SSQ + (2 * j + kh) * 8192 + tok * 4, __float_as_uint(ssq));
    if (j == 0 && kh == 0 && fq == 1) st4_dev(rW, WS_MEAN2 + tok * 4, __float_as_uint(mean2));
    STAMP(16);
    asm volatile("s_waitcnt vmcnt(0) lgkmcnt(0)\n\ts_barrier" ::: "memory");
    if (tid == 0) st8_dev(rW, WS_F3 + (t64 * 8 + j) * 64, (u32x2){tag0, tag1});
    STAMP(17);
  }

  const int fs = xcd * 2 + (r & 1), mh = r >> 1;
  {
    const rsrc_t rD = mkrs(wd, 576 * 4608 * 2);
    const int wm = w & 1, wn = w >> 1;
    const bool lo = w < 4;
    unsigned vb0, vb1, vbx;
    auto mkb = [&](int pi, bool left) AI {
      const int c = pi * 64 + lane, rho = c / 36, cs = c - rho * 36;
      const int row = left ? 72 * ((rho >> 2) & 3) + 64 + 4 * (rho >> 4) + (rho & 3) : rho;
      return (unsigned)(row * 9216 + ((((cs >> 1) ^ ((rho >> 2) & 1)) << 1) + (cs & 1)) * 16 + fs * 576);
    };
    vb0 = mkb(w, false); vb1 = mkb(w + 8, false); vbx = mkb(16 + (w & 1), false);
    const rsrc_t rXt = w < 2 ? rG : rU;
    const unsigned wlx = lds0 + (w < 2 ? F_G : F_U) + (16 + (w & 1)) * 1024;
    const unsigned vA = WS_HCF + mh * 18 * 8192 + tid * 16;
    const unsigned vd = (unsigned)((fs * 288 + w * 8 + (lane >> 3)) * 1152 + (((((lane & 7) >> 1) ^ (fq & 3)) << 1) + (lane & 1)) * 16);
    auto p1 = [&](auto sc, int i) AI {
      constexpr int s = decltype(sc)::v, so = (s % 3) * F_STG;
      constexpr int go = (s < 16 ? 72 * (s >> 1) + 32 * (s & 1) : 288 * (s - 16)) * 9216;
      if (i == 0) dma<so + F_G, go>(rG, vb0, wl1);
      if (i == 1) dma<so + F_U, go>(rU, vb0, wl1);
      if (i == 2) dma_dev<so, s * 8192>(rW, vA, wl1);
      if (i == 3) dma<so + F_G + 8192, go>(rG, vb1, wl1);
      if (i == 4) dma<so + F_U + 8192, go>(rU, vb1, wl1);
      if (i == 5) dma<so, go>(rXt, vbx, wlx);
    };
    auto p2 = [&](auto cc, auto ic) AI {
      constexpr int c = decltype(cc)::v, i = decltype(ic)::v;
      dma<f_wslot(c & 3) + i * 8192, c * 128 + i * 64 * 1152>(rD, vd, wl1);
    };
    auto slot = [&](auto stc, auto slc) AI {
      constexpr int st = decltype(stc)::v, sl = decltype(slc)::v;
      if constexpr (st < 18) {
        constexpr int pl = sl == 0 ? 0 : sl == 2 ? 1 : sl == 3 ? 2 : sl == 5 ? 3 : sl == 6 ? 4 : sl == 8 ? 5 : -1;
        constexpr int ph = sl == 1 ? 0 : sl == 2 ? 1 : sl == 4 ? 2 : sl == 5 ? 3 : sl == 7 ? 4 : -1;
        if constexpr (pl >= 0 && pl == ph) p1(stc, pl);
        else if constexpr (pl >= 0) { if (lo) p1(stc, pl); }
        else if constexpr (ph >= 0) { if (!lo) p1(stc, ph); }
      } else if constexpr (st < 27) {
        if constexpr ((sl & 1) == 0) { if (lo) p2(IC<st - 18>{}, IC<(sl >> 1)>{}); }
        else { if (!lo) p2(IC<st - 18>{}, IC<(sl >> 1)>{}); }
      }
    };
    static_for<0, 3>([&](auto sc) AI {
      p1(sc, 0); p1(sc, 1); p1(sc, 3); p1(sc, 4);
      if (lo) p1(sc, 5);
    });
    if (w == 0) poll(rW, WS_F3 + (mh * 16 + fr) * 64, tag0, tag1, 0);
    asm volatile("s_barrier" ::: "memory");
    STAMP(18);
    dma_dev<F_SSQ, 0>(rW, (unsigned)(WS_SSQ + (2 * w + (lane >> 5)) * 8192 + mh * 512 + (lane & 31) * 16), wl1);
    p1(IC<0>{}, 2); p1(IC<1>{}, 2); p1(IC<2>{}, 2);
    {
      const int li = min((r * 8 + w) * 21 + min(lane, 20), 5183);
      touch<F_RS + 1024>(rD, (unsigned)(xcd * 576 * 1152 + li * 128), lds0 + w * 256);
    }
    const int gb = fq & 1;
    const int rowb = (4 * fq + q4) * 576;
    lptr pE[3], pO[3], pX[3], pA[2];
    pE[0] = L + F_G + rowb + 128 * wn + 32 * gb + 8 * pp;
    pO[0] = L + F_G + rowb + 128 * wn - 32 * gb + 8 * pp;
    pX[0] = L + rowb + (pp < 2 ? F_G : F_U) + ((16 + (wn >> 1)) ^ gb) * 32 + 16 * (wn & 1) + 8 * (pp & 1);
    pA[0] = L + (wm * 4) * 1024 + lane * 16;
    pE[1] = pE[0] + F_STG; pO[1] = pO[0] + F_STG; pE[2] = pE[0] + 2 * F_STG; pO[2] = pO[0] + 2 * F_STG;
    pX[1] = pX[0]; pX[2] = pX[0] + 2 * F_STG; pA[1] = pA[0] + 2 * F_STG;
    PIN(pE[1]); PIN(pO[1]); PIN(pE[2]); PIN(pO[2]); PIN(pX[2]); PIN(pA[1]);

    f32x4 acc[9][4];
    bf16x8 A[2][4], B[9];
    auto rdA = [&](int s, int m) AI { A[s & 1][m] = ldfrag((s % 3 == 2 ? pA[1] : pA[0] + (s % 3) * F_STG) + m * 1024); };
    auto rdB = [&](int s, int n) AI {
      const int b = s % 3;
      if (n < 8) {
        const int nn = n >> 1, off = ((n & 1) ? F_U - F_G : 0) + 32 * nn;
        const lptr q = (nn & 1) ? pO[b] : pE[b];
        B[n] = cat8(trd(q + off), trd(q + off + 16 * 576));
      } else {
        const lptr q = pX[b] + (b == 1 ? F_STG : 0);
        B[n] = cat8(trd(q), trd(q + 16 * 576));
      }
    };
    auto grh = [&](int s, int n, int h) AI {
#pragma unroll
      for (int m = 2 * h; m < 2 * h + 2; ++m) {
        if (s == 0) MFMA_NEW(acc[n][m], B[n], A[s & 1][m]); else MFMA_ACC(acc[n][m], B[n], A[s & 1][m]);
      }
      SBAR();
    };
    wait_bar<3>();
    STAMP(19);
#pragma unroll
    for (int m = 0; m < 4; ++m) rdA(0, m);
#pragma unroll
    for (int n = 0; n < 8; ++n) rdB(0, n);
    if (tid < 128) {
      float s = 0.f;
#pragma unroll
      for (int i = 0; i < 16; ++i) s += *(LAS float*)(L + F_SSQ + i * 512 + tid * 4);
      *(LAS float*)(L + F_RS + tid * 4) = __builtin_amdgcn_rsqf(s * (1.f / 576.f) + 1e-5f);
    }
    SBAR();
    float rs[4];
    static_for<0, 18>([&](auto ic) AI {
      constexpr int s = decltype(ic)::v;
      constexpr bool nx = s < 17;
      using SP = IC<(s >= 1 ? s + 2 : 99)>;
      using SN = IC<(s <= 16 ? s + 3 : 99)>;
      grh(s, 0, 0); slot(SP{}, IC<6>{}); rdB(s, 8); SBAR(); grh(s, 0, 1);
      grh(s, 1, 0); slot(SP{}, IC<7>{}); SBAR(); grh(s, 1, 1);
      grh(s, 2, 0); slot(SP{}, IC<8>{}); SBAR(); grh(s, 2, 1);
      if (s == 0) wait_bar<2>(); else if (s == 1) wait_bar<6>(); else if (s <= 15) wait_bar<5>(); else if (s == 16) wait_bar<4>(); else lds_bar();
      if constexpr (s == 13) { vb0 = mkb(w, true); vb1 = mkb(w + 8, true); vbx = mkb(16 + (w & 1), true); SBAR(); }
      grh(s, 3, 0); slot(SN{}, IC<0>{}); if (nx) { rdA(s + 1, 0); rdA(s + 1, 1); rdB(s + 1, 0); } SBAR(); grh(s, 3, 1);
      grh(s, 4, 0); slot(SN{}, IC<1>{}); if (nx) { rdA(s + 1, 2); rdA(s + 1, 3); rdB(s + 1, 1); } SBAR(); grh(s, 4, 1);
      grh(s, 5, 0); slot(SN{}, IC<2>{}); if (nx) { rdB(s + 1, 2); rdB(s + 1, 3); } SBAR(); grh(s, 5, 1);
      grh(s, 6, 0); slot(SN{}, IC<3>{}); if (nx) { rdB(s + 1, 4); rdB(s + 1, 5); } SBAR(); grh(s, 6, 1);
      grh(s, 7, 0); slot(SN{}, IC<4>{}); if (nx) { rdB(s + 1, 6); }
      if (s == 16) {
#pragma unroll
        for (int m = 0; m < 4; ++m) rs[m] = *(LAS float*)(L + F_RS + (wm * 64 + m * 16 + fr) * 4);
      }
      SBAR(); grh(s, 7, 1);
      grh(s, 8, 0); slot(SN{}, IC<5>{}); SBAR(); grh(s, 8, 1); if (nx) { rdB(s + 1, 7); } SBAR();
    });
    asm volatile("s_nop 7\n\ts_nop 1" ::: "memory");
    SBAR();
    STAMP(20);
    {
      const lptr hb = L + F_H + (wm * 4) * 1024;
      auto glu = [&](float g, float u, float rsv, float rnv) AI { return __builtin_amdgcn_rcpf(1.f + __builtin_amdgcn_exp2f(g * rnv)) * (u * rsv); };
#pragma unroll
      for (int pr = 0; pr < 2; ++pr) {
#pragma unroll
        for (int m = 0; m < 4; ++m) {
          const float rsv = rs[m], rnv = rsv * -1.4426950408889634f;
          const f32x4 g0 = acc[4 * pr][m], u0 = acc[4 * pr + 1][m], g1 = acc[4 * pr + 2][m], u1 = acc[4 * pr + 3][m];
          u32x4 o;
          o[0] = pk2(glu(g0[0], u0[0], rsv, rnv), glu(g0[1], u0[1], rsv, rnv));
          o[1] = pk2(glu(g0[2], u0[2], rsv, rnv), glu(g0[3], u0[3], rsv, rnv));
          o[2] = pk2(glu(g1[0], u1[0], rsv, rnv), glu(g1[1], u1[1], rsv, rnv));
          o[3] = pk2(glu(g1[2], u1[2], rsv, rnv), glu(g1[3], u1[3], rsv, rnv));
          *(LAS u32x4*)(hb + (2 * wn + pr) * 8192 + m * 1024 + lane * 16) = o;
        }
      }
      const bool up = lane >= 32;
#pragma unroll
      for (int mp = 0; mp < 2; ++mp) {
        float g[4], u[4];
#pragma unroll
        for (int rr = 0; rr < 4; ++rr) {
          auto sw = __builtin_amdgcn_permlane32_swap(__float_as_uint(acc[8][2 * mp][rr]), __float_as_uint(acc[8][2 * mp + 1][rr]), false, false);
          g[rr] = __uint_as_float(sw[0]);
          u[rr] = __uint_as_float(sw[1]);
        }
        const float rsv = up ? rs[2 * mp + 1] : rs[2 * mp], rnv = rsv * -1.4426950408889634f;
        const int m = 2 * mp + (up ? 1 : 0);
        const int gam = 2 * wn + gb;
        *(LAS u32x2*)(hb + 8 * 8192 + m * 1024 + (fr + 16 * (gam & 3)) * 16 + (gam >> 2) * 8) =
            (u32x2){pk2(glu(g[0], u[0], rsv, rnv), glu(g[1], u[1], rsv, rnv)), pk2(glu(g[2], u[2], rsv, rnv), glu(g[3], u[3], rsv, rnv))};
      }
    }
    STAMP(21);
    wait_bar<4>();
    lptr pD[4];
    {
      const int rho = 4 * fq + q4;
      pD[0] = L + rho * 128 + ((wn ^ ((rho >> 1) & 3)) * 32) + 8 * pp;
      pD[1] = pD[0] + f_wslot(1); pD[2] = pD[0] + f_wslot(2); pD[3] = pD[0] + f_wslot(3);
      PIN(pD[1]); PIN(pD[2]); PIN(pD[3]);
    }
    auto rdD = [&](int c, int k) AI { B[k] = cat8(trd(pD[c & 3] + k * 4096), trd(pD[c & 3] + k * 4096 + 2048)); };
    bf16x8 HA[9][4];
    {
      const lptr hb = L + F_H + (wm * 4) * 1024 + lane * 16;
      lptr hb2 = hb + 4 * 8192;
      PIN(hb2);
#pragma unroll
      for (int k = 0; k < 9; ++k) {
        if (k < 8) rdD(0, k);
#pragma unroll
        for (int m = 0; m < 4; ++m) HA[k][m] = ldfrag(k < 4 ? hb + k * 8192 + m * 1024 : hb2 + (k - 4) * 8192 + m * 1024);
      }
    }
    const unsigned part = WS_PART + (((fs * 16 + mh) * 9 * 4 + wn) * 2 + wm) * 2048 + lane * 16;
    const unsigned f4 = WS_F4 + (mh * 16 + fs) * 64;
    f32x4 o[2][4];
    auto st2 = [&](int c, int mp) AI {
      const f32x4 a = o[c & 1][2 * mp], b = o[c & 1][2 * mp + 1];
      __builtin_amdgcn_raw_buffer_store_b128((u32x4){pk2(a[0], a[1]), pk2(a[2], a[3]), pk2(b[0], b[1]), pk2(b[2], b[3])}, rW, part, c * 16384 + mp * 1024, 16);
    };
    auto gdh = [&](int c, int k, int h) AI {
#pragma unroll
      for (int m = 2 * h; m < 2 * h + 2; ++m) o[c & 1][m] = MFMA16(B[k], HA[k][m], k == 0 ? z4 : o[c & 1][m]);
      SBAR();
    };
    SBAR();
    auto slot2 = [&](auto ivc, auto slc) AI {
      constexpr int iv = decltype(ivc)::v, sl = decltype(slc)::v;
      if constexpr (iv == 0 || iv == 1) {
        constexpr int bx = 2 + 2 * iv;
        if constexpr (sl < 4) p2(IC<bx>{}, slc);
        else if constexpr (sl == 4) { if (lo) p2(IC<bx>{}, IC<4>{}); else p2(IC<bx + 1>{}, IC<0>{}); }
        else if constexpr (sl < 8) { if (lo) p2(IC<bx + 1>{}, IC<sl - 5>{}); else p2(IC<bx + 1>{}, IC<sl - 4>{}); }
        else { if (lo) { p2(IC<bx + 1>{}, IC<3>{}); p2(IC<bx + 1>{}, IC<4>{}); } }
      } else if constexpr (iv >= 2 && iv <= 4) slot(IC<18 + iv + 4>{}, slc);
    };
    static_for<0, 9>([&](auto ic) AI {
      constexpr int c = decltype(ic)::v;
      constexpr bool nx = c < 8;
      using P = IC<c - 1>;
      gdh(c, 0, 0); slot2(P{}, IC<6>{}); rdD(c, 8); SBAR(); gdh(c, 0, 1);
      gdh(c, 1, 0); slot2(P{}, IC<7>{}); if (c > 0) st2(c - 1, 0); SBAR(); gdh(c, 1, 1);
      gdh(c, 2, 0); slot2(P{}, IC<8>{}); if (c > 0) st2(c - 1, 1); SBAR(); gdh(c, 2, 1);
      wait_bar<(c == 0 ? 0 : c == 1 ? 6 : c == 2 ? 10 : c <= 5 ? 6 : 2)>();
      if constexpr (c >= 2) { if (tid == 0) st8_dev(rW, f4, (u32x2){tag0 + (c - 1), tag1}); }
      gdh(c, 3, 0); slot2(ic, IC<0>{}); if (nx) rdD(c + 1, 0); SBAR(); gdh(c, 3, 1);
      gdh(c, 4, 0); slot2(ic, IC<1>{}); if (nx) rdD(c + 1, 1); SBAR(); gdh(c, 4, 1);
      gdh(c, 5, 0); slot2(ic, IC<2>{}); if (nx) { rdD(c + 1, 2); rdD(c + 1, 3); } SBAR(); gdh(c, 5, 1);
      gdh(c, 6, 0); slot2(ic, IC<3>{}); if (nx) { rdD(c + 1, 4); rdD(c + 1, 5); } SBAR(); gdh(c, 6, 1);
      gdh(c, 7, 0); slot2(ic, IC<4>{}); if (nx) rdD(c + 1, 6); SBAR(); gdh(c, 7, 1);
      gdh(c, 8, 0); slot2(ic, IC<5>{}); SBAR(); gdh(c, 8, 1); if (nx) rdD(c + 1, 7); SBAR();
    });
    st2(8, 0); st2(8, 1);
    STAMP(22);
    asm volatile("s_waitcnt vmcnt(0)\n\ts_barrier" ::: "memory");
    if (tid == 0) st8_dev(rW, f4, (u32x2){tag0 + 9, tag1});
    STAMP(23);
  }

  for (int c = w; c < 9; c += 8) {
    const int mp = fs & 1, wm = (fs >> 1) & 1, wn = fs >> 2;
    const int sub = c * 4 + wn, ms0 = wm * 4 + 2 * mp;
    const int cg = 4 * sub + fq, j = cg / 18, gl = cg - 18 * j;
    const unsigned ho = WS_HCF + ((mh * 18 + (gl < 16 ? 2 * j + (gl >> 3) : 16 + (j >> 2))) * 8 + ms0) * 1024 +
                        (fr + 16 * (gl < 16 ? (gl & 3) : (j & 3))) * 16 + (gl < 16 ? ((gl >> 2) & 1) : gl - 16) * 8;
    poll(rW, WS_F4 + (mh * 16 + fr) * 64, tag0, tag1, c + 1);
    const unsigned vo = WS_PART + ((mh * 9 + c) * 16 + fs) * 1024 + lane * 16;
    u32x4 v[16];
#pragma unroll
    for (int f = 0; f < 16; ++f) v[f] = ld16_dev(rW, vo, f * 2304 * 1024);
    u32x2 hc[2];
    float mu[2];
#pragma unroll
    for (int i = 0; i < 2; ++i) {
      hc[i] = ld8_dev(rW, ho + i * 1024);
      mu[i] = __uint_as_float(ld4_dev(rW, WS_MEAN2 + (mh * 128 + (ms0 + i) * 16 + fr) * 4));
    }
    float s[8];
#pragma unroll
    for (int k = 0; k < 8; ++k) s[k] = 0.f;
#pragma unroll
    for (int f = 0; f < 16; ++f)
#pragma unroll
      for (int k = 0; k < 4; ++k) { s[2 * k] += bflo(v[f][k]); s[2 * k + 1] += bfhi(v[f][k]); }
#pragma unroll
    for (int i = 0; i < 2; ++i) {
      const u32x2 o = {pk2(s[4 * i] + (bflo(hc[i][0]) + mu[i]), s[4 * i + 1] + (bfhi(hc[i][0]) + mu[i])),
                       pk2(s[4 * i + 2] + (bflo(hc[i][1]) + mu[i]), s[4 * i + 3] + (bfhi(hc[i][1]) + mu[i]))};
      *(u32x2*)(out + (size_t)(mh * 128 + (ms0 + i) * 16 + fr) * 576 + sub * 16 + 4 * fq) = o;
    }
    if (c < 8) STAMP(24); else STAMP(25);
  }
}

extern "C" void kernel_launch(void* const* d_in, const int* in_sizes, int n_in, void* d_out, int out_size, void* d_ws, size_t ws_size,
                              hipStream_t stream) {
  mk_block<<<256, 512, 0, stream>>>((const u16*)d_in[0], (const u16*)d_in[1], (const u16*)d_in[2], (const u16*)d_in[4],
                                    (const u16*)d_in[5], (const u16*)d_in[6], (char*)d_ws, (u16*)d_out);
}
```

```cpp
#include <hip/hip_runtime.h>
#include <hip/hip_bf16.h>
#include <stdint.h>

#define PUBSTEP 8
#define ABL 0
typedef unsigned short u16;
typedef short bf16x8 __attribute__((ext_vector_type(8)));
typedef short s16x4 __attribute__((ext_vector_type(4)));
typedef float f32x4 __attribute__((ext_vector_type(4)));
typedef float f32x2 __attribute__((ext_vector_type(2)));
typedef unsigned u32x4 __attribute__((ext_vector_type(4)));
typedef unsigned u32x2 __attribute__((ext_vector_type(2)));
typedef __bf16 bf16x2_t __attribute__((ext_vector_type(2)));
typedef __amdgpu_buffer_rsrc_t rsrc_t;
#define LAS __attribute__((address_space(3)))
#define DI __device__ __forceinline__
#define AI __attribute__((always_inline))
typedef LAS char* lptr;

#define MFMA16(a, b, c) __builtin_amdgcn_mfma_f32_16x16x32_bf16((a), (b), (c), 0, 0, 0)
#define SBAR() __builtin_amdgcn_sched_barrier(0)
#define PIN(x) asm volatile("" : "+v"(x))
#define MFMA_ACC(c, a, b) asm("v_mfma_f32_16x16x32_bf16 %0, %1, %2, %0" : "+v"(c) : "v"(a), "v"(b))
#define MFMA_NEW(c, a, b) asm("v_mfma_f32_16x16x32_bf16 %0, %1, %2, 0" : "=&v"(c) : "v"(a), "v"(b))

constexpr int REC = 3328;
constexpr int WS_EPOCH = 0;
constexpr int WS_F1 = 256;
constexpr int WS_F2 = WS_F1 + 256 * 64;
constexpr int WS_F3 = WS_F2 + 256 * 64;
constexpr int WS_F4 = WS_F3 + 256 * 64;
constexpr int WS_REC = WS_F4 + 256 * 64;
constexpr int WS_MEAN1 = WS_REC + 256 * REC;
constexpr int WS_AF = WS_MEAN1 + 8192;
constexpr int WS_WEF = WS_AF + 2048 * 576 * 2;
constexpr int WS_RMF = WS_WEF + 8 * 18 * 4608;
constexpr int WS_HCF = WS_RMF + 8 * 2048;
constexpr int WS_SSQ = WS_HCF + 2048 * 576 * 2;
constexpr int WS_MEAN2 = WS_SSQ + 16 * 8192;
constexpr int WS_PART = WS_MEAN2 + 8192;
constexpr int WS_END = WS_PART + 16 * 2048 * 576 * 2;

template <int I> struct IC { static constexpr int v = I; };
template <int I, int N, class Fn> DI void static_for(Fn&& f) {
  if constexpr (I < N) { f(IC<I>{}); static_for<I + 1, N>(f); }
}
DI unsigned pk2(float a, float b) {
  bf16x2_t v = {(__bf16)a, (__bf16)b};
  return __builtin_bit_cast(unsigned, v);
}
DI float bflo(unsigned w) { return __uint_as_float(w << 16); }
DI float bfhi(unsigned w) { return __uint_as_float(w & 0xffff0000u); }
DI s16x4 trd(lptr p) { return __builtin_bit_cast(s16x4, __builtin_amdgcn_ds_read_tr16_b64_v4i16((LAS s16x4*)p)); }
DI bf16x8 cat8(s16x4 lo, s16x4 hi) { return __builtin_shufflevector(lo, hi, 0, 1, 2, 3, 4, 5, 6, 7); }
DI bf16x8 ldfrag(lptr p) { return *(LAS bf16x8*)p; }
DI rsrc_t mkrs(const void* p, int bytes) { return __builtin_amdgcn_make_buffer_rsrc((void*)p, (short)0, bytes, 0x00020000); }
template <int LOFF, int GOFF> DI void dma(rsrc_t rs, unsigned voff, unsigned wl) {
  unsigned t;
  asm volatile("s_add_u32 m0, %3, %4\n\ts_mov_b32 %0, %5\n\tbuffer_load_dwordx4 %1, %2, %0 offen lds"
               : "=&s"(t) : "v"(voff), "s"(rs), "s"(wl), "i"(LOFF), "i"(GOFF) : "memory", "scc");
}
template <int LOFF, int GOFF> DI void dma_dev(rsrc_t rs, unsigned voff, unsigned wl) {
  unsigned t;
  asm volatile("s_add_u32 m0, %3, %4\n\ts_mov_b32 %0, %5\n\tbuffer_load_dwordx4 %1, %2, %0 offen sc1 lds"
               : "=&s"(t) : "v"(voff), "s"(rs), "s"(wl), "i"(LOFF), "i"(GOFF) : "memory", "scc");
}
DI void dma_dyn(rsrc_t rs, unsigned voff, unsigned soff, unsigned ldst) {
  asm volatile("s_mov_b32 m0, %3\n\ts_nop 0\n\tbuffer_load_dwordx4 %0, %1, %2 offen sc1 lds" ::"v"(voff), "s"(rs), "s"(soff), "s"(ldst) : "memory");
}
template <int LOFF> DI void touch(rsrc_t rs, unsigned voff, unsigned wl) {
  asm volatile("s_add_u32 m0, %2, %3\n\ts_nop 0\n\tbuffer_load_dword %0, %1, 0 offen lds" ::"v"(voff), "s"(rs), "s"(wl), "i"(LOFF) : "memory", "scc");
}
DI void st16_dev(rsrc_t rs, unsigned voff, u32x4 v) { __builtin_amdgcn_raw_buffer_store_b128(v, rs, voff, 0, 16); }
DI void st8_dev(rsrc_t rs, unsigned voff, u32x2 v) { __builtin_amdgcn_raw_buffer_store_b64(v, rs, voff, 0, 16); }
DI void st4_dev(rsrc_t rs, unsigned voff, unsigned v) { __builtin_amdgcn_raw_buffer_store_b32(v, rs, voff, 0, 16); }
DI u32x4 ld16_dev(rsrc_t rs, unsigned voff, int soff) { return __builtin_amdgcn_raw_buffer_load_b128(rs, voff, soff, 16); }
DI u32x2 ld8_dev(rsrc_t rs, unsigned voff) { return __builtin_amdgcn_raw_buffer_load_b64(rs, voff, 0, 16); }
DI unsigned ld4_dev(rsrc_t rs, unsigned voff) { return __builtin_amdgcn_raw_buffer_load_b32(rs, voff, 0, 16); }
template <int N> DI void wait_bar() { asm volatile("s_waitcnt vmcnt(%0) lgkmcnt(0)\n\ts_barrier" ::"i"(N) : "memory"); }
DI void lds_bar() { asm volatile("s_waitcnt lgkmcnt(0)\n\ts_barrier" ::: "memory"); }
DI float phi_f(float t) { return (t > 0.f ? t + 1.f : __builtin_amdgcn_exp2f(t * 1.4426950408889634f)) + 1e-6f; }
DI float xor16(float v) { auto r = __builtin_amdgcn_permlane16_swap(__float_as_uint(v), __float_as_uint(v), false, false); return __uint_as_float(r[0]) + __uint_as_float(r[1]); }
DI float xor32(float v) { auto r = __builtin_amdgcn_permlane32_swap(__float_as_uint(v), __float_as_uint(v), false, false); return __uint_as_float(r[0]) + __uint_as_float(r[1]); }
DI float sum_fq(float v) { return xor32(xor16(v)); }
DI void poll(rsrc_t rs, unsigned voff, unsigned t0, unsigned t1, unsigned need) {
#pragma unroll 1
  for (unsigned spin = 0; spin < (1u << 17); ++spin) {
    u32x2 f;
    asm volatile("buffer_load_dwordx2 %0, %1, %2, 0 offen sc1\n\ts_waitcnt vmcnt(0)" : "=v"(f) : "v"(voff), "s"(rs) : "memory");
    const unsigned d = f[0] - t0;
    if (__all(f[1] == t1 && d >= need && d < 16u)) break;
    __builtin_amdgcn_s_sleep(1);
  }
}

#define STAMP_DECL
#define STAMP(i) do {} while (0)

constexpr int KA_SLOT = 16384 + 10240;
constexpr int KA_KVO = 16384;
constexpr int ka_q(int kb) { return kb < 5 ? 4 * KA_SLOT + kb * 5120 : (kb - 5) * 5120; }
constexpr int KA_EX = KA_SLOT;
constexpr int KA_QC = 2 * KA_SLOT;
constexpr int KA_WO = 5 * KA_SLOT;
constexpr int KA_CS = KA_WO + 3072;
constexpr int KA_KVI = KA_CS + 8 * 320;
constexpr int KA_RSP = KA_KVI + 6144;
constexpr int KA_ZZ = KA_RSP + 160;
constexpr int KA_RS = KA_ZZ + 160;
constexpr int KA_DEAD = KA_RS + 320;
static_assert(KA_DEAD + 2048 <= 163840, "lds");

struct Tab { unsigned v[1024]; };
constexpr Tab make_tab() {
  Tab T{};
  for (int t = 0; t < 512; ++t) {
    const int w = t >> 6, ln = t & 63, fr = ln & 15, fq = ln >> 4;
    const int slot = w == 0 ? ln : w == 3 ? 64 + ln : w == 1 ? (fr < 2 ? 128 + fq * 2 + fr : -1) : w == 2 ? (fq == 0 ? 136 + fr : -1)
                   : w == 4 ? (fr == 8 ? 152 + fq : (ln < 8 ? 197 + ln : ln == 9 ? 205 : -1)) : w == 5 ? ((fr >= 4 && fr <= 8) ? 156 + fq * 5 + fr - 4 : -1)
                   : w == 6 ? (fq == 0 ? 176 + fr : -1) : ((fq == 0 && fr >= 4 && fr <= 8) ? 192 + fr - 4 : -1);
    int ce = 0, cd = 0;
    if (t < 128) { ce = (t & 15) + (t >> 6) * 16; cd = ((t >> 4) & 3) * 4 + (t >> 6) * 16; }
    else if (t < 136) { ce = 16 + (t & 1); cd = ((t - 128) >> 1) * 4; }
    else if (t < 152) { ce = t - 136; cd = 16; }
    else if (t < 156) { ce = 36; cd = (t - 152) * 4; }
    else if (t < 176) { const int i = t - 156, f = i / 5; ce = 32 + i - f * 5; cd = 16 + 4 * f; }
    else if (t < 192) { ce = t - 160; cd = 32; }
    else if (t < 197) { ce = 32 + t - 192; cd = 32; }
    int m = 0;
    for (int rr = 0; rr < 4; ++rr) m |= ((cd + rr >= 18) == (ce >= 18)) ? (1 << rr) : 0;
    int dst = KA_KVI + ce * 96 + cd * 2;
    if (ce == 36) { dst = KA_ZZ + cd * 4; m = 16; }
    if (t >= 197) { dst = KA_RS + (t < 206 ? (t - 197) * 32 : 0); m = t < 206 ? 32 : 64; }
    T.v[2 * t] = slot >= 0 ? (unsigned)slot * 16u : 0x40000000u;
    T.v[2 * t + 1] = (unsigned)dst | ((unsigned)m << 24);
  }
  return T;
}
static __device__ const Tab g_tab = make_tab();

constexpr int KB_B = 18 * 4096;
constexpr int KB_RM = KB_B + 18 * 4608;
constexpr int F_G = 8192, F_U = 8192 + 18432 + 16, F_STG = F_U + 18432;
constexpr int F_SSQ = 3 * F_STG;
constexpr int F_RS = F_SSQ + 8192;
constexpr int F_H = 163840 - 73728;
constexpr int R8A = 32768, R8B = F_STG + 32768;
DI unsigned r_home(unsigned b, unsigned f) {
  const unsigned base = b < 2 ? F_STG : b < 4 ? F_H : b < 6 ? F_H + 36864 : 0;
  return b < 8 ? base + (b & 1) * 16384 + f * 1024 : (f < 8 ? R8A : R8B - 8192) + f * 1024;
}
constexpr int f_wslot(int i) { return i == 0 ? 0 : i == 1 ? F_STG : i == 2 ? F_H : F_H + 36864; }

__global__ __launch_bounds__(512, 2) void mk_block(const u16* __restrict__ x, const u16* __restrict__ wqkv, const u16* __restrict__ wo,
                                                   const u16* __restrict__ wg, const u16* __restrict__ wu, const u16* __restrict__ wd,
                                                   char* __restrict__ ws, u16* __restrict__ out) {
  __shared__ __attribute__((aligned(16))) char smem[163840];
  STAMP_DECL; STAMP(0);
  const lptr L = (lptr)smem;
  const unsigned lds0 = (unsigned)(uintptr_t)L;
  const int tid = threadIdx.x, lane = tid & 63, w = __builtin_amdgcn_readfirstlane(tid >> 6);
  const int fr = lane & 15, fq = lane >> 4, q4 = fr >> 2, pp = lane & 3;
  const int xcd = blockIdx.x & 7, r = blockIdx.x >> 3;
  const unsigned wl1 = lds0 + w * 1024, wl2 = lds0 + w * 2048;
  const rsrc_t rW = mkrs(ws, WS_END), rX = mkrs(x, 2048 * 576 * 2);
  const rsrc_t rG = mkrs(wg, 576 * 4608 * 2), rU = mkrs(wu, 576 * 4608 * 2);
  const f32x4 z4 = {0.f, 0.f, 0.f, 0.f};
  unsigned tag0, tag1;
  const int gw = r * 8 + w;
  auto warm_gu = [&](auto lc, int row0, int nl) AI {
    const int li = gw * nl + min(lane, nl - 1), row = li / 9;
    const unsigned off = (unsigned)((row0 + row) * 9216 + (li - row * 9) * 128 + xcd * 1152);
    touch<decltype(lc)::v>(rG, off, lds0 + w * 256);
    touch<decltype(lc)::v>(rU, off, lds0 + w * 256);
  };

  {
    const rsrc_t rQ = mkrs(wqkv, 576 * 1728 * 2);
    const int tt = (xcd & 3) * 4 + (r & 3), p = (xcd >> 2) * 8 + (r >> 2);
    const int o8 = (p & 1) * 8, ot = (tt & 1) * 8;
    const int tok0 = tt * 128 + w * 16;
    const unsigned vx = (unsigned)((tok0 + (lane >> 3)) * 1152 + (((lane & 7) ^ (lane >> 3)) * 16));
    {
      const unsigned g = blockIdx.x * 8 + w;
      touch<KA_DEAD>(rX, lane < 9 ? (g * 9 + lane) * 128 : 0x7fffff00u, lds0 + w * 256);
      touch<KA_DEAD>(rQ, lane < 8 ? (g * 8 + lane) * 128 : 0x7fffff00u, lds0 + w * 256);
    }
    dma<0, 0>(rX, vx, wl2);
    dma<1024, 8 * 1152>(rX, vx, wl2);
    auto mkv = [&](int pi) AI {
      const int c = pi * 64 + lane, row = c / 10, cc = c - row * 10, rho = row & 31;
      const int k = (row & 32) + ((rho >> 2) & 3) * 8 + (rho >> 4) * 4 + (rho & 3);
      return (unsigned)(k * 3456 + (cc >= 5 ? 1152 - 80 : 0) + cc * 16 + 1152 + 72 * p - o8);
    };
    const unsigned vkv0 = mkv(w), vkv8 = mkv(8 + (w & 1));
    const bool xtra = w >= 6;
    const unsigned wlx = lds0 + (w & 1) * 1024;
    auto piece = [&](auto kbc, int i) AI {
      constexpr int kb = decltype(kbc)::v, so = (kb % 5) * KA_SLOT;
      if ((ABL == 2 || ABL == 9) && kb >= 3) return;
      if (i == 0) dma<so, kb * 128>(rX, vx, wl2);
      if (i == 1) dma<so + 1024, kb * 128 + 8 * 1152>(rX, vx, wl2);
      if (i == 2) dma<so + KA_KVO, kb * 64 * 3456>(rQ, vkv0, wl1);
      if (i == 3) { if (xtra) dma<so + KA_KVO + 8192, kb * 64 * 3456>(rQ, vkv8, wlx); }
    };
    piece(IC<0>{}, 2); piece(IC<0>{}, 3);
    piece(IC<1>{}, 0); piece(IC<1>{}, 1); piece(IC<1>{}, 2); piece(IC<1>{}, 3);
    u32x2 ep, tab;
    asm volatile("buffer_load_dwordx2 %0, off, %1, 0 sc1" : "=v"(ep) : "s"(rW) : "memory");
    asm volatile("global_load_dwordx2 %0, %1, %2" : "=v"(tab) : "v"(tid * 8), "s"(g_tab.v) : "memory");
    if (w < 3) {
      const int c = min(tid, 179), rw = c / 5;
      dma<KA_WO, 0>(mkrs(wo, 576 * 576 * 2), (unsigned)((36 * p + rw) * 1152 + 72 * tt - ot + (c - rw * 5) * 16), wl1);
    }
    piece(IC<2>{}, 0); piece(IC<2>{}, 1); piece(IC<2>{}, 2); piece(IC<2>{}, 3);
    if (tid < 384) *(LAS u32x4*)(L + KA_KVI + tid * 16) = (u32x4){0u, 0u, 0u, 0u};
    unsigned vq;
    {
      const int c = (w < 5 ? w : 0) * 64 + lane, row = c / 5, ch = c - row * 5;
      const int rho = row & 31, rr = rho & 15;
      vq = (unsigned)(((row & 32) + 8 * (2 * (rr & 1) + (rr >> 3)) + 4 * (rho >> 4) + ((rr >> 1) & 3)) * 3456 + ch * 16 + 72 * p - o8);
    }
    lptr bk[3], bx[3], ba0[3], ba1[3];
    bk[0] = L + KA_KVO + (4 * fq + q4) * 160 + o8 + 8 * pp;
    bx[0] = L + KA_KVO + (4 * fq + q4) * 160 + 80 * (pp & 1) + o8 + 64;
    ba0[0] = L + w * 2048 + fr * 128 + (fq ^ (fr & 7)) * 16;
    ba1[0] = L + w * 2048 + fr * 128 + ((4 + fq) ^ (fr & 7)) * 16;
#pragma unroll
    for (int i = 1; i < 3; ++i) {
      bk[i] = bk[0] + 2 * i * KA_SLOT; bx[i] = bx[0] + 2 * i * KA_SLOT; ba0[i] = ba0[0] + 2 * i * KA_SLOT; ba1[i] = ba1[0] + 2 * i * KA_SLOT;
      PIN(bk[i]); PIN(bx[i]); PIN(ba0[i]); PIN(ba1[i]);
    }
    f32x4 acc[5], cs[5], gram = z4, sx = z4;
#pragma unroll
    for (int i = 0; i < 5; ++i) acc[i] = cs[i] = z4;
    bf16x8 ones = {0x3F80, 0x3F80, 0x3F80, 0x3F80, 0x3F80, 0x3F80, 0x3F80, 0x3F80};
    PIN(ones);
    bf16x8 a[18], B[2][5];
    auto readA = [&](int kb) AI {
      const int sl = kb % 5;
      a[2 * kb] = ldfrag(ba0[sl >> 1] + (sl & 1) * KA_SLOT);
      a[2 * kb + 1] = ldfrag(ba1[sl >> 1] + (sl & 1) * KA_SLOT);
    };
    auto readB = [&](int s, int i) AI {
      const int sl = (s >> 1) % 5, so = (sl & 1) * KA_SLOT + (s & 1) * 5120;
      if ((ABL == 3 || ABL == 9) && s >= 2) return;
      if (i < 4) {
        const int off = so + (i >> 1) * 80 + 32 * (i & 1);
        B[s & 1][i] = cat8(trd(bk[sl >> 1] + off), trd(bk[sl >> 1] + off + 2560));
      } else B[s & 1][4] = cat8(trd(bx[sl >> 1] + so), trd(bx[sl >> 1] + so + 2560));
    };
    auto issue_q = [&](auto kc) AI {
      constexpr int k = decltype(kc)::v;
      dma<ka_q(k), k * 64 * 3456>(rQ, vq, wl1);
    };
    auto slot = [&](auto kbc, auto slc) AI {
      constexpr int KB = decltype(kbc)::v, sl = decltype(slc)::v;
      if constexpr (KB <= 0) {
        constexpr int b = KB < 0 ? 3 : (sl < 3 ? 4 : 5);
        piece(IC<b>{}, sl % 3);
        if constexpr (sl % 3 == 2) piece(IC<b>{}, 3);
      }
      if constexpr (KB >= 1 && KB <= 3 && sl < 4) piece(IC<KB + 5>{}, sl);
      if constexpr (KB == 4 && sl < 5) { if (w < 5) issue_q(IC<sl>{}); }
      if constexpr (KB == 5 && sl < 4) { if (w < 5) issue_q(IC<5 + sl>{}); }
    };
    auto step = [&](auto sc, auto kbc, auto s0c) AI {
      constexpr int s = decltype(sc)::v, s0 = decltype(s0c)::v;
      constexpr bool nx = s < 17;
      acc[0] = MFMA16(B[s & 1][0], a[s], acc[0]); acc[1] = MFMA16(B[s & 1][1], a[s], acc[1]); SBAR();
      if constexpr (nx && (s & 1)) readA((s + 1) >> 1);
      if constexpr (nx) { readB(s + 1, 0); readB(s + 1, 1); }
      slot(kbc, IC<s0>{}); SBAR();
      acc[2] = MFMA16(B[s & 1][2], a[s], acc[2]); acc[3] = MFMA16(B[s & 1][3], a[s], acc[3]); SBAR();
      if constexpr (nx) { readB(s + 1, 2); readB(s + 1, 3); }
      slot(kbc, IC<s0 + 1>{}); SBAR();
      acc[4] = MFMA16(B[s & 1][4], a[s], acc[4]); gram = MFMA16(a[s], a[s], gram); sx = MFMA16(ones, a[s], sx); SBAR();
      if constexpr (nx) readB(s + 1, 4);
      slot(kbc, IC<s0 + 2>{}); SBAR();
      if (s >= 15 || (w & 1) == (s & 1)) {
#pragma unroll
        for (int i = 0; i < 5; ++i) cs[i] = MFMA16(B[s & 1][i], ones, cs[i]);
      }
      SBAR();
    };
    STAMP(1);
    wait_bar<8>();
    STAMP(2);
    readA(0);
#pragma unroll
    for (int i = 0; i < 5; ++i) readB(0, i);
    f32x4 csp[5];
    static_for<0, 9>([&](auto ic) AI {
      constexpr int KB = decltype(ic)::v;
      step(IC<2 * KB>{}, IC<KB - 1>{}, IC<3>{});
      if constexpr (KB == 2) {
        if (tid < 36) {
          float s = 0.f;
#pragma unroll
          for (int i = 0; i < 9; ++i) {
            const u32x2 v = *(LAS u32x2*)(L + KA_WO + tid * 80 + ot + i * 8);
            s += (bflo(v[0]) + bfhi(v[0])) + (bflo(v[1]) + bfhi(v[1]));
          }
          *(LAS float*)(L + KA_RSP + tid * 4) = s;
        }
      }
      if constexpr (KB == 7) {
        if (fr == 0) {
#pragma unroll
          for (int i = 0; i < 5; ++i) *(LAS f32x4*)(L + KA_CS + w * 320 + (i * 4 + fq) * 16) = cs[i];
        }
      }
      if constexpr (KB == 0) wait_bar<8>();
      else if constexpr (KB <= 4) wait_bar<9>();
      else if constexpr (KB == 5) { if (w < 5) wait_bar<11>(); else wait_bar<6>(); }
      else if constexpr (KB == 6) { if (w < 5) wait_bar<12>(); else wait_bar<3>(); }
      else if constexpr (KB == 7) { if (w < 5) wait_bar<9>(); else wait_bar<0>(); }
      if constexpr (KB == 7) {
#pragma unroll
        for (int i = 0; i < 5; ++i) csp[i] = *(LAS f32x4*)(L + KA_CS + (w ^ 1) * 320 + (i * 4 + fq) * 16);
      }
      step(IC<2 * KB + 1>{}, ic, IC<0>{});
    });
    STAMP(3);
    float meanT, rstdT;
    {
      const int rr = fr & 3;
      float g = rr == 0 ? gram[0] : rr == 1 ? gram[1] : rr == 2 ? gram[2] : gram[3];
      g = sum_fq(q4 == fq ? g : 0.f);
      meanT = sx[0] * (1.f / 576.f);
      rstdT = __builtin_amdgcn_rsqf(fmaxf(g * (1.f / 576.f) - meanT * meanT, 0.f) + 1e-5f);
      const lptr exw = L + KA_EX + (w * 16 + fr) * 160 + fq * 8;
#pragma unroll
      for (int i = 0; i < 5; ++i) {
        float v[4];
#pragma unroll
        for (int k = 0; k < 4; ++k) {
          const float t = (acc[i][k] - meanT * (cs[i][k] + csp[i][k])) * rstdT;
          if (i < 2) v[k] = phi_f(t);
          else if (i < 4) v[k] = t;
          else { const float ph = phi_f(t); v[k] = fq == 0 ? ph : (fq == 1 ? t : 1.0f); }
        }
        *(LAS u32x2*)(exw + i * 32) = (u32x2){pk2(v[0], v[1]), pk2(v[2], v[3])};
      }
    }
    STAMP(4);
    asm volatile("s_waitcnt vmcnt(0) lgkmcnt(0)\n\ts_barrier" : "+v"(ep), "+v"(tab)::"memory");
    const unsigned recb = WS_REC + p * 16 * REC;
    {
      const int sa = (0x44101100 >> (4 * w)) & 7, sb = (0x43443232 >> (4 * w)) & 7;
      const lptr ex = L + KA_EX + (4 * fq + q4) * 160 + pp * 8;
      const lptr exa = ex + sa * 32, exb = ex + sb * 32;
      bf16x8 fa[4], fb[4];
#pragma unroll
      for (int k = 0; k < 4; ++k) {
        fa[k] = cat8(trd(exa + k * 5120), trd(exa + k * 5120 + 2560));
        fb[k] = cat8(trd(exb + k * 5120), trd(exb + k * 5120 + 2560));
      }
      const f32x4 rsp = *(LAS f32x4*)(L + KA_RSP + min(lane, 8) * 16);
      f32x4 T = z4;
#pragma unroll
      for (int k = 0; k < 4; ++k) T = MFMA16(fa[k], fb[k], T);
      if (tab[0] >= 197 * 16) T = rsp;
      st16_dev(rW, recb + tt * REC + tab[0], __builtin_bit_cast(u32x4, T));
    }
    STAMP(5);
    tag0 = __builtin_amdgcn_readfirstlane((ep[0] * 0x9E3779B1u + 0x7F4A7C15u + ep[1]) & ~15u);
    tag1 = __builtin_amdgcn_readfirstlane((ep[1] ^ (ep[0] >> 7)) * 0x85EBCA6Bu + 0x165667B1u);
    if (tag1 == (unsigned)__builtin_amdgcn_readfirstlane(ep[1])) tag1 ^= 0x5bd1e995u;
    f32x4 qa[3];
    {
#pragma unroll
      for (int i = 0; i < 3; ++i) qa[i] = z4;
      lptr bq[2], bqp[2];
      bq[0] = L + (2 * (4 * (fq & 1) + q4) + (fq >> 1)) * 80 + o8 + 64;
      bqp[0] = bq[0] - 64 + 8 * pp;
      bq[1] = bq[0] + 4 * KA_SLOT; bqp[1] = bqp[0] + 4 * KA_SLOT;
      PIN(bq[1]); PIN(bqp[1]);
      bf16x8 b[3][3];
      auto rq = [&](int s) AI {
        const int h = s < 10 ? 1 : 0, o = (s < 10 ? s : s - 10) * 2560;
        b[s % 3][0] = cat8(trd(bqp[h] + o), trd(bqp[h] + o + 1280));
        b[s % 3][1] = cat8(trd(bqp[h] + o + 32), trd(bqp[h] + o + 1280 + 32));
        b[s % 3][2] = cat8(trd(bq[h] + o), trd(bq[h] + o + 1280));
      };
      if (w >= 4) {
        f32x4 qc[3] = {z4, z4, z4};
        const int j = w & 3;
#pragma unroll
        for (int i = 0; i < 3; ++i) {
          const int kb = j + 4 * i, o = kb < 5 ? 4 * KA_SLOT + kb * 5120 : (kb - 5) * 5120;
          if (i < 2 || j == 0) {
#pragma unroll
            for (int h = 0; h < 2; ++h) {
              const lptr q0 = bqp[0] + o + h * 2560, q1 = bq[0] + o + h * 2560;
              qc[0] = MFMA16(cat8(trd(q0), trd(q0 + 1280)), ones, qc[0]);
              qc[1] = MFMA16(cat8(trd(q0 + 32), trd(q0 + 1280 + 32)), ones, qc[1]);
              qc[2] = MFMA16(cat8(trd(q1), trd(q1 + 1280)), ones, qc[2]);
            }
          }
        }
        if (fr == 0) {
#pragma unroll
          for (int i = 0; i < 3; ++i) *(LAS f32x4*)(L + KA_QC + ((i * 4 + fq) * 4 + j) * 16) = qc[i];
        }
        asm volatile("s_waitcnt vmcnt(0) lgkmcnt(0)\n\ts_barrier" ::: "memory");
        const unsigned off = (unsigned)((r * 4 + j) * 9216 + min(lane, 8) * 128 + xcd * 1152);
        touch<KA_DEAD>(rG, off, lds0 + w * 256);
        touch<KA_DEAD>(rU, off, lds0 + w * 256);
      }
      rq(0); rq(1);
      static_for<0, 18>([&](auto ic) AI {
        constexpr int s = decltype(ic)::v;
        if constexpr (s < 16) rq(s + 2);
#pragma unroll
        for (int i = 0; i < 3; ++i) qa[i] = MFMA16(b[s % 3][i], a[s], qa[i]);
        SBAR();
        if constexpr (s == PUBSTEP) {
          if (w < 4) {
            asm volatile("s_waitcnt vmcnt(0)\n\ts_barrier" ::: "memory");
            if (tid == 0) st8_dev(rW, WS_F1 + (p * 16 + tt) * 64, (u32x2){tag0, tag1});
            STAMP(6);
          }
        }
      });
      if (p == 0 && fq == 0) st4_dev(rW, WS_MEAN1 + (tok0 + fr) * 4, __float_as_uint(meanT));
      float pq[3][4];
#pragma unroll
      for (int i = 0; i < 3; ++i) {
        const lptr qp = L + KA_QC + (i * 4 + fq) * 64;
        const f32x4 cq = (*(LAS f32x4*)qp + *(LAS f32x4*)(qp + 16)) + (*(LAS f32x4*)(qp + 32) + *(LAS f32x4*)(qp + 48));
#pragma unroll
        for (int k = 0; k < 4; ++k) { pq[i][k] = phi_f((qa[i][k] - meanT * cq[k]) * rstdT); PIN(pq[i][k]); }
      }
      STAMP(7);
      const int ct = w < 3 ? w : w < 6 ? w - 3 : 0;
      lptr wa = L + KA_KVI + (4 * fq + q4) * 96 + (w < 3 ? 0 : 64) + pp * 8;
      lptr wb[4];
#pragma unroll
      for (int tq = 0; tq < 4; ++tq) {
        const int ec = tq < 2 ? 16 * tq + 4 * fq + q4 : min(16 * tq + 4 * fq + q4, 35);
        wb[tq] = (ct == 2 && pp > 0) ? L + KA_RS + ec * 8 : L + KA_WO + ec * 80 + ot + ct * 32 + pp * 8;
        PIN(wb[tq]);
      }
      unsigned wst;
      {
        const int c36 = 16 * ct + fr, c72 = (tt & 1) * 36 + c36, sub = c72 >> 4, frc = c72 & 15;
        const int ks = w < 3 ? p : 16 + (p >> 3), fqq = w < 3 ? fq : (p & 3), hb = w < 3 ? 0 : ((p >> 2) & 1) * 8;
        wst = WS_WEF + ((tt >> 1) * 18 + ks) * 4608 + (sub < 4 ? sub * 1024 + (frc + 16 * fqq) * 16 : 4096 + (frc + 8 * fqq) * 16) + hb;
        if (c36 == 36) wst = WS_RMF + (tt >> 1) * 2048 + (ks * 4 + fqq) * 16 + hb;
        if (c36 > 36 || (c36 == 36 && (tt & 1)) || (w >= 3 && fq != 0) || w >= 6) wst = 0x40000000u;
      }
      PIN(wa); PIN(wst);
      const unsigned afb = WS_AF + (tok0 >> 6) * 18 * 4096 + ((tok0 >> 4) & 3) * 1024;
      u32x4 v[16];
      if (w < 4) {
        STAMP(8);
        poll(rW, WS_F1 + (p * 16 + fr) * 64, tag0, tag1, 0);
        STAMP(9);
        const unsigned vo = recb + min(tid, 205) * 16;
#pragma unroll
        for (int t = 0; t < 16; ++t) v[t] = ld16_dev(rW, vo, t * REC);
      }
      if (w < 4) {
#define BC(i) __builtin_bit_cast(f32x4, v[i])
        const f32x4 s = (((BC(0) + BC(1)) + (BC(2) + BC(3))) + ((BC(4) + BC(5)) + (BC(6) + BC(7)))) +
                        (((BC(8) + BC(9)) + (BC(10) + BC(11))) + ((BC(12) + BC(13)) + (BC(14) + BC(15))));
#undef BC
        const unsigned cm = tab[1] >> 24;
        const lptr cd = L + (tab[1] & 0xffffff);
        if (cm < 16) {
          *(LAS u32x2*)cd = (u32x2){pk2((cm & 1) ? s[0] : 0.f, (cm & 2) ? s[1] : 0.f), pk2((cm & 4) ? s[2] : 0.f, (cm & 8) ? s[3] : 0.f)};
        } else if (cm == 16) {
          *(LAS f32x4*)cd = s;
        } else if (cm == 32) {
          const f32x4 t = s * (1.f / 576.f);
          *(LAS u32x4*)cd = (u32x4){pk2(t[0], 0.f), 0u, pk2(t[1], 0.f), 0u};
          *(LAS u32x4*)(cd + 16) = (u32x4){pk2(t[2], 0.f), 0u, pk2(t[3], 0.f), 0u};
        }
      }
      lds_bar();
      STAMP(10);
      {
        bf16x8 fa[2][2], fb[2];
#pragma unroll
        for (int k = 0; k < 2; ++k) {
          fa[0][k] = cat8(trd(wa + k * 3072), trd(wa + k * 3072 + 1536));
          fa[1][k] = cat8(trd(wa + k * 3072 + 32), trd(wa + k * 3072 + 1536 + 32));
          fb[k] = cat8(trd(wb[2 * k]), trd(wb[2 * k + 1]));
        }
        f32x4 zz[3];
#pragma unroll
        for (int i = 0; i < 3; ++i) zz[i] = *(LAS f32x4*)(L + KA_ZZ + (i < 2 ? 16 * i + 4 * fq : 32) * 4);
        const f32x4 T0 = MFMA16(fa[0][1], fb[1], MFMA16(fa[0][0], fb[0], z4));
        const f32x4 T1 = MFMA16(fa[1][1], fb[1], MFMA16(fa[1][0], fb[0], z4));
        float d0 = (pq[0][0] * zz[0][0] + pq[0][1] * zz[0][1]) + (pq[0][2] * zz[0][2] + pq[0][3] * zz[0][3]);
        const float lo2 = pq[1][0] * zz[1][0] + pq[1][1] * zz[1][1];
        float d1 = pq[1][2] * zz[1][2] + pq[1][3] * zz[1][3];
        const float x2 = (pq[2][0] * zz[2][0] + pq[2][1] * zz[2][1]) + (pq[2][2] * zz[2][2] + pq[2][3] * zz[2][3]);
        d0 += fq == 0 ? lo2 : 0.f;
        d1 += fq == 0 ? x2 : lo2;
        d0 = sum_fq(d0);
        d1 = sum_fq(d1);
        const float i0 = __builtin_amdgcn_rcpf(d0 + 1e-6f), i1 = __builtin_amdgcn_rcpf(d1 + 1e-6f), il = fq == 0 ? i0 : i1;
        st16_dev(rW, afb + p * 4096 + lane * 16,
                 (u32x4){pk2(pq[0][0] * i0, pq[0][1] * i0), pk2(pq[0][2] * i0, pq[0][3] * i0), pk2(pq[1][0] * il, pq[1][1] * il), pk2(pq[1][2] * i1, pq[1][3] * i1)});
        if (fq == 0)
          st8_dev(rW, afb + (16 + (p >> 3)) * 4096 + (fr + 16 * (p & 3)) * 16 + ((p >> 2) & 1) * 8,
                  (u32x2){pk2(pq[2][0] * i1, pq[2][1] * i1), pk2(pq[2][2] * i1, pq[2][3] * i1)});
        const u32x4 wv = {pk2(T0[0], T0[1]), pk2(T0[2], T0[3]), pk2(T1[0], T1[1]), pk2(T1[2], T1[3])};
        if (w < 3) st16_dev(rW, wst, wv); else st8_dev(rW, wst, (u32x2){wv[0], wv[1]});
      }
    }
    STAMP(11);
    asm volatile("s_waitcnt vmcnt(0) lgkmcnt(0)\n\ts_barrier" ::: "memory");
    if (tid == 0) st8_dev(rW, WS_F2 + (p * 16 + tt) * 64, (u32x2){tag0, tag1});
    STAMP(12);
  }

  {
    const int t64 = (xcd & 3) * 8 + (r & 7), j = (xcd >> 2) * 4 + (r >> 3);
    const int msub = w & 3, kh = w >> 2;
    const int tok = t64 * 64 + msub * 16 + fr;
    u32x2 xv[3];
    {
      const char* xr = (const char*)x + (size_t)tok * 1152 + 144 * j;
      xv[0] = *(const u32x2*)(xr + kh * 64 + 8 * fq);
      xv[1] = *(const u32x2*)(xr + kh * 64 + 32 + 8 * fq);
      xv[2] = *(const u32x2*)(xr + 128 + 8 * (fq & 1));
    }
    if (w == 0) {
      const int i = lane < 48 ? lane : 0;
      poll(rW, WS_F2 + (i < 16 ? i * 16 + (t64 >> 1) : ((i - 16) >> 1) * 16 + 2 * j + (i & 1)) * 64, tag0, tag1, 0);
      if (blockIdx.x == 0 && lane == 0) st8_dev(rW, WS_EPOCH, (u32x2){tag0, tag1});
    }
    asm volatile("s_barrier" ::: "memory");
    STAMP(13);
    const unsigned va = WS_AF + t64 * 18 * 4096 + tid * 16, vb = WS_WEF + j * 18 * 4608 + tid * 16;
    if (w == 3 || w == 4) dma_dev<KB_RM - 3 * 1024, 0>(rW, WS_RMF + j * 2048 + tid * 16 - 3 * 1024, wl1);
    const unsigned m1 = ld4_dev(rW, WS_MEAN1 + tok * 4);
    static_for<0, 3>([&](auto gc) AI {
      constexpr int g = decltype(gc)::v;
      dma_dev<g * 24576, g * 24576>(rW, va, wl1);
      dma_dev<KB_B + g * 27648, g * 27648>(rW, vb, wl1);
      dma_dev<g * 24576 + 8192, g * 24576 + 8192>(rW, va, wl1);
      dma_dev<KB_B + g * 27648 + 8192, g * 27648 + 8192>(rW, vb, wl1);
      dma_dev<g * 24576 + 16384, g * 24576 + 16384>(rW, va, wl1);
      dma_dev<KB_B + g * 27648 + 16384, g * 27648 + 16384>(rW, vb, wl1);
      if (w < 3) dma_dev<KB_B + g * 27648 + 24576, g * 27648 + 24576>(rW, vb, wl1);
    });
    warm_gu(IC<161792>{}, 128, 8);
    SBAR();
    f32x4 acc[5];
#pragma unroll
    for (int i = 0; i < 5; ++i) acc[i] = z4;
    const lptr pa = L + kh * 4096 + msub * 1024 + lane * 16;
    lptr pb[2];
    pb[0] = L + KB_B + kh * 4608 + lane * 16;
    pb[1] = pb[0] + 5 * 9216;
    PIN(pb[1]);
    lptr p4 = fr < 8 ? L + KB_B + kh * 4608 + 4096 + (fr + 8 * fq) * 16 : L + KB_RM + (kh * 4 + fq) * 16;
    const int p4s = fr < 8 ? 9216 : 128;
    static_for<0, 3>([&](auto gc) AI {
      constexpr int g = decltype(gc)::v;
      wait_bar<6 * (2 - g) + 2>();
      if constexpr (g == 0) STAMP(14);
      bf16x8 av[3], bv[3][5];
#pragma unroll
      for (int k = 0; k < 3; ++k) {
        const int kb = 3 * g + k;
        av[k] = ldfrag(pa + kb * 8192);
#pragma unroll
        for (int i = 0; i < 4; ++i) bv[k][i] = ldfrag((kb < 5 ? pb[0] + kb * 9216 : pb[1] + (kb - 5) * 9216) + i * 1024);
        bv[k][4] = ldfrag(p4);
        p4 += p4s;
      }
#pragma unroll
      for (int k = 0; k < 3; ++k)
#pragma unroll
        for (int i = 0; i < 5; ++i) acc[i] = MFMA16(bv[k][i], av[k], acc[i]);
    });
    STAMP(15);
    lds_bar();
    {
      const lptr xs = L + (msub * 2 + kh) * 3072 + lane * 16;
      *(LAS f32x4*)(xs) = kh == 0 ? acc[2] : acc[0];
      *(LAS f32x4*)(xs + 1024) = kh == 0 ? acc[3] : acc[1];
      *(LAS f32x4*)(xs + 2048) = acc[4];
    }
    lds_bar();
    f32x4 o[3];
    {
      const lptr xs = L + (msub * 2 + (1 - kh)) * 3072 + lane * 16;
      o[0] = (kh == 0 ? acc[0] : acc[2]) + *(LAS f32x4*)(xs);
      o[1] = (kh == 0 ? acc[1] : acc[3]) + *(LAS f32x4*)(xs + 1024);
      o[2] = acc[4] + *(LAS f32x4*)(xs + 2048);
    }
    const float mean2 = __uint_as_float(m1) + sum_fq(fq == 2 ? o[2][0] : 0.f);
    u32x2 pk[3];
    float ssq = 0.f;
#pragma unroll
    for (int i = 0; i < 3; ++i) {
      pk[i] = (u32x2){pk2(bflo(xv[i][0]) + o[i][0] - mean2, bfhi(xv[i][0]) + o[i][1] - mean2),
                      pk2(bflo(xv[i][1]) + o[i][2] - mean2, bfhi(xv[i][1]) + o[i][3] - mean2)};
      const float a0 = bflo(pk[i][0]), a1 = bfhi(pk[i][0]), a2 = bflo(pk[i][1]), a3 = bfhi(pk[i][1]);
      const float q = (a0 * a0 + a1 * a1) + (a2 * a2 + a3 * a3);
      ssq += (i < 2 || (kh == 0 && fq < 2)) ? q : 0.f;
    }
    ssq = sum_fq(ssq);
    const unsigned hb = WS_HCF + ((tok >> 7) * 18 * 8 + ((tok >> 4) & 7)) * 1024;
    st16_dev(rW, hb + (2 * j + kh) * 8192 + lane * 16, (u32x4){pk[0][0], pk[0][1], pk[1][0], pk[1][1]});
    if (kh == 0 && fq < 2) st8_dev(rW, hb + (16 + (j >> 2)) * 8192 + (fr + 16 * (j & 3)) * 16 + fq * 8, pk[2]);
    if (fq == 0) st4_dev(rW, WS_SSQ + (2 * j + kh) * 8192 + tok * 4, __float_as_uint(ssq));
    if (j == 0 && kh == 0 && fq == 1) st4_dev(rW, WS_MEAN2 + tok * 4, __float_as_uint(mean2));
    STAMP(16);
    asm volatile("s_waitcnt vmcnt(0) lgkmcnt(0)\n\ts_barrier" ::: "memory");
    if (tid == 0) st8_dev(rW, WS_F3 + (t64 * 8 + j) * 64, (u32x2){tag0, tag1});
    STAMP(17);
  }

  const int fs = xcd * 2 + (r & 1), mh = r >> 1;
  unsigned nb = 0;
  const unsigned rfl = WS_F4 + (mh * 16 + 2 * w + (lane & 1)) * 64;
  const unsigned rvo = WS_PART + ((2 * w * 16 + mh) * 9 * 16 + fs) * 1024 + lane * 16;
  auto progress = [&](u32x2 f) AI {
    unsigned d = f[0] - tag0;
    d = (f[1] == tag1 && d < 16u) ? d : 0u;
    return min((unsigned)__builtin_amdgcn_readlane(d, 0), (unsigned)__builtin_amdgcn_readlane(d, 1));
  };
  {
    const rsrc_t rD = mkrs(wd, 576 * 4608 * 2);
    const int wm = w & 1, wn = w >> 1;
    const bool lo = w < 4;
    unsigned vb0, vb1, vbx;
    auto mkb = [&](int pi, bool left) AI {
      const int c = pi * 64 + lane, rho = c / 36, cs = c - rho * 36;
      const int row = left ? 72 * ((rho >> 2) & 3) + 64 + 4 * (rho >> 4) + (rho & 3) : rho;
      return (unsigned)(row * 9216 + ((((cs >> 1) ^ ((rho >> 2) & 1)) << 1) + (cs & 1)) * 16 + fs * 576);
    };
    vb0 = mkb(w, false); vb1 = mkb(w + 8, false); vbx = mkb(16 + (w & 1), false);
    const rsrc_t rXt = w < 2 ? rG : rU;
    const unsigned wlx = lds0 + (w < 2 ? F_G : F_U) + (16 + (w & 1)) * 1024;
    const unsigned vA = WS_HCF + mh * 18 * 8192 + tid * 16;
    const unsigned vd = (unsigned)((fs * 288 + w * 8 + (lane >> 3)) * 1152 + (((((lane & 7) >> 1) ^ (fq & 3)) << 1) + (lane & 1)) * 16);
    auto p1 = [&](auto sc, int i) AI {
      constexpr int s = decltype(sc)::v, so = (s % 3) * F_STG;
      constexpr int go = (s < 16 ? 72 * (s >> 1) + 32 * (s & 1) : 288 * (s - 16)) * 9216;
      if (i == 0) dma<so + F_G, go>(rG, vb0, wl1);
      if (i == 1) dma<so + F_U, go>(rU, vb0, wl1);
      if (i == 2) dma_dev<so, s * 8192>(rW, vA, wl1);
      if (i == 3) dma<so + F_G + 8192, go>(rG, vb1, wl1);
      if (i == 4) dma<so + F_U + 8192, go>(rU, vb1, wl1);
      if (i == 5) dma<so, go>(rXt, vbx, wlx);
    };
    auto p2 = [&](auto cc, auto ic) AI {
      constexpr int c = decltype(cc)::v, i = decltype(ic)::v;
      dma<f_wslot(c & 3) + i * 8192, c * 128 + i * 64 * 1152>(rD, vd, wl1);
    };
    auto slot = [&](auto stc, auto slc) AI {
      constexpr int st = decltype(stc)::v, sl = decltype(slc)::v;
      if constexpr (st < 18) {
        constexpr int pl = sl == 0 ? 0 : sl == 2 ? 1 : sl == 3 ? 2 : sl == 5 ? 3 : sl == 6 ? 4 : sl == 8 ? 5 : -1;
        constexpr int ph = sl == 1 ? 0 : sl == 2 ? 1 : sl == 4 ? 2 : sl == 5 ? 3 : sl == 7 ? 4 : -1;
        if constexpr (pl >= 0 && pl == ph) p1(stc, pl);
        else if constexpr (pl >= 0) { if (lo) p1(stc, pl); }
        else if constexpr (ph >= 0) { if (!lo) p1(stc, ph); }
      } else if constexpr (st < 27) {
        if constexpr ((sl & 1) == 0) { if (lo) p2(IC<st - 18>{}, IC<(sl >> 1)>{}); }
        else { if (!lo) p2(IC<st - 18>{}, IC<(sl >> 1)>{}); }
      }
    };
    auto wts = [&]() AI {
      static_for<0, 3>([&](auto sc) AI {
        p1(sc, 0); p1(sc, 1); p1(sc, 3); p1(sc, 4);
        if (lo) p1(sc, 5);
      });
    };
    if (w == (mh & 7)) { poll(rW, WS_F3 + (mh * 16 + fr) * 64, tag0, tag1, 0); asm volatile("s_barrier" ::: "memory"); wts(); }
    else { wts(); asm volatile("s_barrier" ::: "memory"); }
    STAMP(18);
    dma_dev<F_SSQ, 0>(rW, (unsigned)(WS_SSQ + (2 * w + (lane >> 5)) * 8192 + mh * 512 + (lane & 31) * 16), wl1);
    p1(IC<0>{}, 2); p1(IC<1>{}, 2); p1(IC<2>{}, 2);
    const int gb = fq & 1;
    const int rowb = (4 * fq + q4) * 576;
    lptr pE[3], pO[3], pX[3], pA[2];
    pE[0] = L + F_G + rowb + 128 * wn + 32 * gb + 8 * pp;
    pO[0] = L + F_G + rowb + 128 * wn - 32 * gb + 8 * pp;
    pX[0] = L + rowb + (pp < 2 ? F_G : F_U) + ((16 + (wn >> 1)) ^ gb) * 32 + 16 * (wn & 1) + 8 * (pp & 1);
    pA[0] = L + (wm * 4) * 1024 + lane * 16;
    pE[1] = pE[0] + F_STG; pO[1] = pO[0] + F_STG; pE[2] = pE[0] + 2 * F_STG; pO[2] = pO[0] + 2 * F_STG;
    pX[1] = pX[0]; pX[2] = pX[0] + 2 * F_STG; pA[1] = pA[0] + 2 * F_STG;
    PIN(pE[1]); PIN(pO[1]); PIN(pE[2]); PIN(pO[2]); PIN(pX[2]); PIN(pA[1]);

    f32x4 acc[9][4];
    bf16x8 A[2][4], B[9];
    auto rdA = [&](int s, int m) AI { A[s & 1][m] = ldfrag((s % 3 == 2 ? pA[1] : pA[0] + (s % 3) * F_STG) + m * 1024); };
    auto rdB = [&](int s, int n) AI {
      const int b = s % 3;
      if (n < 8) {
        const int nn = n >> 1, off = ((n & 1) ? F_U - F_G : 0) + 32 * nn;
        const lptr q = (nn & 1) ? pO[b] : pE[b];
        B[n] = cat8(trd(q + off), trd(q + off + 16 * 576));
      } else {
        const lptr q = pX[b] + (b == 1 ? F_STG : 0);
        B[n] = cat8(trd(q), trd(q + 16 * 576));
      }
    };
    auto grh = [&](int s, int n, int h) AI {
#pragma unroll
      for (int m = 2 * h; m < 2 * h + 2; ++m) {
        if (s == 0) MFMA_NEW(acc[n][m], B[n], A[s & 1][m]); else MFMA_ACC(acc[n][m], B[n], A[s & 1][m]);
      }
      SBAR();
    };
    wait_bar<2>();
    STAMP(19);
#pragma unroll
    for (int m = 0; m < 4; ++m) rdA(0, m);
#pragma unroll
    for (int n = 0; n < 8; ++n) rdB(0, n);
    if (tid < 128) {
      float s = 0.f;
#pragma unroll
      for (int i = 0; i < 16; ++i) s += *(LAS float*)(L + F_SSQ + i * 512 + tid * 4);
      *(LAS float*)(L + F_RS + tid * 4) = __builtin_amdgcn_rsqf(s * (1.f / 576.f) + 1e-5f);
    }
    SBAR();
    float rs[4];
    static_for<0, 18>([&](auto ic) AI {
      constexpr int s = decltype(ic)::v;
      constexpr bool nx = s < 17;
      using SP = IC<(s >= 1 ? s + 2 : 99)>;
      using SN = IC<(s <= 16 ? s + 3 : 99)>;
      grh(s, 0, 0); slot(SP{}, IC<6>{}); rdB(s, 8); SBAR(); grh(s, 0, 1);
      grh(s, 1, 0); slot(SP{}, IC<7>{}); SBAR(); grh(s, 1, 1);
      grh(s, 2, 0); slot(SP{}, IC<8>{}); SBAR(); grh(s, 2, 1);
      if (s == 0) wait_bar<1>(); else if (s == 3) wait_bar<7>(); else if (s == 7) wait_bar<6>(); else if (s <= 15) wait_bar<5>(); else if (s == 16) wait_bar<4>(); else lds_bar();
      if constexpr (s == 2) { warm_gu(IC<F_RS + 1024>{}, 352, 8); SBAR(); }
      if constexpr (s == 6) {
        touch<F_RS + 1024>(rD, (unsigned)(xcd * 576 * 1152 + min(gw * 21 + min(lane, 20), 5183) * 128), lds0 + w * 256);
        SBAR();
      }
      if constexpr (s == 13) { vb0 = mkb(w, true); vb1 = mkb(w + 8, true); vbx = mkb(16 + (w & 1), true); SBAR(); }
      grh(s, 3, 0); slot(SN{}, IC<0>{}); if (nx) { rdA(s + 1, 0); rdA(s + 1, 1); rdB(s + 1, 0); } SBAR(); grh(s, 3, 1);
      grh(s, 4, 0); slot(SN{}, IC<1>{}); if (nx) { rdA(s + 1, 2); rdA(s + 1, 3); rdB(s + 1, 1); } SBAR(); grh(s, 4, 1);
      grh(s, 5, 0); slot(SN{}, IC<2>{}); if (nx) { rdB(s + 1, 2); rdB(s + 1, 3); } SBAR(); grh(s, 5, 1);
      grh(s, 6, 0); slot(SN{}, IC<3>{}); if (nx) { rdB(s + 1, 4); rdB(s + 1, 5); } SBAR(); grh(s, 6, 1);
      grh(s, 7, 0); slot(SN{}, IC<4>{}); if (nx) { rdB(s + 1, 6); }
      if (s == 16) {
#pragma unroll
        for (int m = 0; m < 4; ++m) rs[m] = *(LAS float*)(L + F_RS + (wm * 64 + m * 16 + fr) * 4);
      }
      SBAR(); grh(s, 7, 1);
      grh(s, 8, 0); slot(SN{}, IC<5>{}); SBAR(); grh(s, 8, 1); if (nx) { rdB(s + 1, 7); } SBAR();
    });
    asm volatile("s_nop 7\n\ts_nop 1" ::: "memory");
    SBAR();
    STAMP(20);
    {
      const lptr hb = L + F_H + (wm * 4) * 1024;
      auto glu = [&](float g, float u, float rsv, float rnv) AI { return __builtin_amdgcn_rcpf(1.f + __builtin_amdgcn_exp2f(g * rnv)) * (u * rsv); };
#pragma unroll
      for (int pr = 0; pr < 2; ++pr) {
#pragma unroll
        for (int m = 0; m < 4; ++m) {
          const float rsv = rs[m], rnv = rsv * -1.4426950408889634f;
          const f32x4 g0 = acc[4 * pr][m], u0 = acc[4 * pr + 1][m], g1 = acc[4 * pr + 2][m], u1 = acc[4 * pr + 3][m];
          u32x4 o;
          o[0] = pk2(glu(g0[0], u0[0], rsv, rnv), glu(g0[1], u0[1], rsv, rnv));
          o[1] = pk2(glu(g0[2], u0[2], rsv, rnv), glu(g0[3], u0[3], rsv, rnv));
          o[2] = pk2(glu(g1[0], u1[0], rsv, rnv), glu(g1[1], u1[1], rsv, rnv));
          o[3] = pk2(glu(g1[2], u1[2], rsv, rnv), glu(g1[3], u1[3], rsv, rnv));
          *(LAS u32x4*)(hb + (2 * wn + pr) * 8192 + m * 1024 + lane * 16) = o;
        }
      }
      const bool up = lane >= 32;
#pragma unroll
      for (int mp = 0; mp < 2; ++mp) {
        float g[4], u[4];
#pragma unroll
        for (int rr = 0; rr < 4; ++rr) {
          auto sw = __builtin_amdgcn_permlane32_swap(__float_as_uint(acc[8][2 * mp][rr]), __float_as_uint(acc[8][2 * mp + 1][rr]), false, false);
          g[rr] = __uint_as_float(sw[0]);
          u[rr] = __uint_as_float(sw[1]);
        }
        const float rsv = up ? rs[2 * mp + 1] : rs[2 * mp], rnv = rsv * -1.4426950408889634f;
        const int m = 2 * mp + (up ? 1 : 0);
        const int gam = 2 * wn + gb;
        *(LAS u32x2*)(hb + 8 * 8192 + m * 1024 + (fr + 16 * (gam & 3)) * 16 + (gam >> 2) * 8) =
            (u32x2){pk2(glu(g[0], u[0], rsv, rnv), glu(g[1], u[1], rsv, rnv)), pk2(glu(g[2], u[2], rsv, rnv), glu(g[3], u[3], rsv, rnv))};
      }
    }
    STAMP(21);
    wait_bar<4>();
    lptr pD[4];
    {
      const int rho = 4 * fq + q4;
      pD[0] = L + rho * 128 + ((wn ^ ((rho >> 1) & 3)) * 32) + 8 * pp;
      pD[1] = pD[0] + f_wslot(1); pD[2] = pD[0] + f_wslot(2); pD[3] = pD[0] + f_wslot(3);
      PIN(pD[1]); PIN(pD[2]); PIN(pD[3]);
    }
    auto rdD = [&](int c, int k) AI { B[k] = cat8(trd(pD[c & 3] + k * 4096), trd(pD[c & 3] + k * 4096 + 2048)); };
    bf16x8 HA[9][4];
    {
      const lptr hb = L + F_H + (wm * 4) * 1024 + lane * 16;
      lptr hb2 = hb + 4 * 8192;
      PIN(hb2);
#pragma unroll
      for (int k = 0; k < 9; ++k) {
        if (k < 8) rdD(0, k);
#pragma unroll
        for (int m = 0; m < 4; ++m) HA[k][m] = ldfrag(k < 4 ? hb + k * 8192 + m * 1024 : hb2 + (k - 4) * 8192 + m * 1024);
      }
    }
    const unsigned part = WS_PART + (((fs * 16 + mh) * 9 * 4 + wn) * 2 + wm) * 2048 + lane * 16;
    const unsigned f4 = WS_F4 + (mh * 16 + fs) * 64;
    f32x4 o[2][4];
    auto st2 = [&](int c, int mp) AI {
      const f32x4 a = o[c & 1][2 * mp], b = o[c & 1][2 * mp + 1];
      __builtin_amdgcn_raw_buffer_store_b128((u32x4){pk2(a[0], a[1]), pk2(a[2], a[3]), pk2(b[0], b[1]), pk2(b[2], b[3])}, rW, part, c * 16384 + mp * 1024, 16);
    };
    auto gdh = [&](int c, int k, int h) AI {
#pragma unroll
      for (int m = 2 * h; m < 2 * h + 2; ++m) o[c & 1][m] = MFMA16(B[k], HA[k][m], k == 0 ? z4 : o[c & 1][m]);
      SBAR();
    };
    SBAR();
    auto slot2 = [&](auto ivc, auto slc) AI {
      constexpr int iv = decltype(ivc)::v, sl = decltype(slc)::v;
      if constexpr (iv == 0 || iv == 1) {
        constexpr int bx = 2 + 2 * iv;
        if constexpr (sl < 4) p2(IC<bx>{}, slc);
        else if constexpr (sl == 4) { if (lo) p2(IC<bx>{}, IC<4>{}); else p2(IC<bx + 1>{}, IC<0>{}); }
        else if constexpr (sl < 8) { if (lo) p2(IC<bx + 1>{}, IC<sl - 5>{}); else p2(IC<bx + 1>{}, IC<sl - 4>{}); }
        else { if (lo) { p2(IC<bx + 1>{}, IC<3>{}); p2(IC<bx + 1>{}, IC<4>{}); } }
      } else if constexpr (iv >= 2 && iv <= 4) slot(IC<18 + iv + 4>{}, slc);
    };
    u32x2 pf;
    auto flag_ld = [&]() AI { asm volatile("buffer_load_dwordx2 %0, %1, %2, 0 offen sc1" : "=v"(pf) : "v"(rfl), "s"(rW) : "memory"); };
    auto fetch = [&](unsigned lim) AI {
      const bool ok = nb < lim;
      const unsigned so = ok ? nb * 16384u : 0x40000000u, ld = lds0 + (ok ? r_home(nb, 2 * w) : (unsigned)R8B);
      dma_dyn(rW, rvo, so, ld);
      dma_dyn(rW, rvo + 2304 * 1024, so, ld + 1024);
      nb += ok ? 1u : 0u;
    };
    unsigned lim = 0;
    static_for<0, 9>([&](auto ic) AI {
      constexpr int c = decltype(ic)::v;
      constexpr bool nx = c < 8;
      using P = IC<c - 1>;
      gdh(c, 0, 0); slot2(P{}, IC<6>{}); rdD(c, 8); SBAR(); gdh(c, 0, 1);
      gdh(c, 1, 0); slot2(P{}, IC<7>{}); if (c > 0) st2(c - 1, 0); SBAR(); gdh(c, 1, 1);
      gdh(c, 2, 0); slot2(P{}, IC<8>{}); if (c > 0) st2(c - 1, 1); SBAR(); gdh(c, 2, 1);
      if constexpr (c >= 5) asm volatile("s_waitcnt vmcnt(6) lgkmcnt(0)\n\ts_barrier" : "+v"(pf)::"memory");
      else wait_bar<(c == 0 ? 0 : c == 1 ? 6 : c == 2 ? 10 : 6)>();
      if constexpr (c >= 2) { if (tid == 0) st8_dev(rW, f4, (u32x2){tag0 + (c - 1), tag1}); }
      gdh(c, 3, 0);
      if constexpr (c >= 5) lim = min(progress(pf), (unsigned)(c == 5 ? 2 : c == 6 ? 4 : 6));
      if constexpr (c >= 4 && c <= 7) flag_ld();
      slot2(ic, IC<0>{}); if (nx) rdD(c + 1, 0); SBAR(); gdh(c, 3, 1);
      gdh(c, 4, 0); slot2(ic, IC<1>{}); if constexpr (c >= 5) fetch(lim); if (nx) rdD(c + 1, 1); SBAR(); gdh(c, 4, 1);
      gdh(c, 5, 0); slot2(ic, IC<2>{}); if (nx) { rdD(c + 1, 2); rdD(c + 1, 3); } SBAR(); gdh(c, 5, 1);
      gdh(c, 6, 0); slot2(ic, IC<3>{}); if constexpr (c >= 5) fetch(lim); if (nx) { rdD(c + 1, 4); rdD(c + 1, 5); } SBAR(); gdh(c, 6, 1);
      gdh(c, 7, 0); slot2(ic, IC<4>{}); if (nx) rdD(c + 1, 6); SBAR(); gdh(c, 7, 1);
      gdh(c, 8, 0); slot2(ic, IC<5>{}); SBAR(); gdh(c, 8, 1); if (nx) rdD(c + 1, 7); SBAR();
    });
    st2(8, 0); st2(8, 1);
    STAMP(22);
    asm volatile("s_waitcnt vmcnt(0)\n\ts_barrier" ::: "memory");
    if (tid == 0) st8_dev(rW, f4, (u32x2){tag0 + 9, tag1});
    STAMP(23);
  }

  {
    const int mp = fs & 1, wm = (fs >> 1) & 1, wn = fs >> 2, ms0 = wm * 4 + 2 * mp;
    u32x2 hc[2];
    float mu[2];
    auto resid = [&](int c) AI {
      const int cg = 4 * (c * 4 + wn) + fq, j = cg / 18, gl = cg - 18 * j;
      const unsigned ho = WS_HCF + ((mh * 18 + (gl < 16 ? 2 * j + (gl >> 3) : 16 + (j >> 2))) * 8 + ms0) * 1024 +
                          (fr + 16 * (gl < 16 ? (gl & 3) : (j & 3))) * 16 + (gl < 16 ? ((gl >> 2) & 1) : gl - 16) * 8;
      hc[0] = ld8_dev(rW, ho);
      hc[1] = ld8_dev(rW, ho + 1024);
    };
    auto fin = [&](int c, const float(&s)[8], int i) AI {
      {
        const u32x2 o = {pk2(s[4 * i] + (bflo(hc[i][0]) + mu[i]), s[4 * i + 1] + (bfhi(hc[i][0]) + mu[i])),
                         pk2(s[4 * i + 2] + (bflo(hc[i][1]) + mu[i]), s[4 * i + 3] + (bfhi(hc[i][1]) + mu[i]))};
        *(u32x2*)(out + (size_t)(mh * 128 + (ms0 + i) * 16 + fr) * 576 + (c * 4 + wn) * 16 + 4 * fq) = o;
      }
    };
    resid(w);
#pragma unroll
    for (int i = 0; i < 2; ++i) mu[i] = __uint_as_float(ld4_dev(rW, WS_MEAN2 + (mh * 128 + (ms0 + i) * 16 + fr) * 4));
#pragma unroll 1
    for (unsigned spin = 0; nb < 9 && spin < (1u << 17); ++spin) {
      u32x2 f;
      asm volatile("buffer_load_dwordx2 %0, %1, %2, 0 offen sc1\n\ts_waitcnt vmcnt(0)" : "=v"(f) : "v"(rfl), "s"(rW) : "memory");
      const unsigned pr = progress(f);
#pragma unroll 1
      for (; nb < pr; ++nb) {
        const unsigned ld = lds0 + r_home(nb, 2 * w);
        dma_dyn(rW, rvo, nb * 16384u, ld);
        dma_dyn(rW, rvo + 2304 * 1024, nb * 16384u, ld + 1024);
      }
    }
    wait_bar<0>();
    STAMP(24);
    float s[8];
#pragma unroll
    for (int k = 0; k < 8; ++k) s[k] = 0.f;
    const lptr rb = L + r_home(w, 0) + lane * 16;
#pragma unroll
    for (int f = 0; f < 16; ++f) {
      const u32x4 v = *(LAS u32x4*)(rb + f * 1024);
#pragma unroll
      for (int k = 0; k < 4; ++k) { s[2 * k] += bflo(v[k]); s[2 * k + 1] += bfhi(v[k]); }
    }
    fin(w, s, 0); fin(w, s, 1);
    if (w < 2) {
      resid(8);
#pragma unroll
      for (int k = 0; k < 8; ++k) s[k] = 0.f;
#pragma unroll
      for (int f = 0; f < 16; ++f) {
        const u32x2 v = *(LAS u32x2*)(L + (f < 8 ? R8A : R8B - 8192) + f * 1024 + lane * 16 + w * 8);
        s[0] += bflo(v[0]); s[1] += bfhi(v[0]); s[2] += bflo(v[1]); s[3] += bfhi(v[1]);
      }
#pragma unroll
      for (int k = 0; k < 4; ++k) s[4 + k] = s[k];
      if (w == 0) fin(8, s, 0); else fin(8, s, 1);
    }
    STAMP(25);
  }
}

extern "C" void kernel_launch(void* const* d_in, const int* in_sizes, int n_in, void* d_out, int out_size, void* d_ws, size_t ws_size,
                              hipStream_t stream) {
  mk_block<<<256, 512, 0, stream>>>((const u16*)d_in[0], (const u16*)d_in[1], (const u16*)d_in[2], (const u16*)d_in[4],
                                    (const u16*)d_in[5], (const u16*)d_in[6], (char*)d_ws, (u16*)d_out);
}
```

```cpp
#include <hip/hip_runtime.h>
#include <hip/hip_bf16.h>
#include <stdint.h>

#define PUBSTEP 8
#define F2V 0
#define LOSL 0
#define HISL 8
#define CODE_BYTES 69120
#define ABL 0
typedef unsigned short u16;
typedef short bf16x8 __attribute__((ext_vector_type(8)));
typedef short s16x4 __attribute__((ext_vector_type(4)));
typedef float f32x4 __attribute__((ext_vector_type(4)));
typedef float f32x2 __attribute__((ext_vector_type(2)));
typedef unsigned u32x4 __attribute__((ext_vector_type(4)));
typedef unsigned u32x2 __attribute__((ext_vector_type(2)));
typedef __bf16 bf16x2_t __attribute__((ext_vector_type(2)));
typedef __amdgpu_buffer_rsrc_t rsrc_t;
#define LAS __attribute__((address_space(3)))
#define DI __device__ __forceinline__
#define AI __attribute__((always_inline))
typedef LAS char* lptr;

#define MFMA16(a, b, c) __builtin_amdgcn_mfma_f32_16x16x32_bf16((a), (b), (c), 0, 0, 0)
#define SBAR() __builtin_amdgcn_sched_barrier(0)
#define PIN(x) asm volatile("" : "+v"(x))
#define MFMA_ACC(c, a, b) asm("v_mfma_f32_16x16x32_bf16 %0, %1, %2, %0" : "+v"(c) : "v"(a), "v"(b))
#define MFMA_NEW(c, a, b) asm("v_mfma_f32_16x16x32_bf16 %0, %1, %2, 0" : "=&v"(c) : "v"(a), "v"(b))

constexpr int REC = 3328;
constexpr int WS_EPOCH = 0;
constexpr int WS_F1 = 256;
constexpr int WS_F2 = WS_F1 + 256 * 64;
constexpr int WS_F3 = WS_F2 + 256 * 64;
constexpr int WS_F4 = WS_F3 + 256 * 64;
constexpr int WS_F5 = WS_F4 + 256 * 64;
constexpr int WS_REC = WS_F5 + 256 * 64;
constexpr int WS_MEAN1 = WS_REC + 256 * REC;
constexpr int WS_AF = WS_MEAN1 + 8192;
constexpr int WS_WEF = WS_AF + 2048 * 576 * 2;
constexpr int WS_RMF = WS_WEF + 8 * 18 * 4608;
constexpr int WS_HCF = WS_RMF + 8 * 2048;
constexpr int WS_SSQ = WS_HCF + 2048 * 576 * 2;
constexpr int WS_MEAN2 = WS_SSQ + 16 * 8192;
constexpr int WS_HX = WS_MEAN2 + 8192;
constexpr int WS_PART = WS_HX + 256 * 36864;
constexpr int WS_END = WS_PART + 8 * 2048 * 576 * 2;

template <int I> struct IC { static constexpr int v = I; };
template <int I, int N, class Fn> DI void static_for(Fn&& f) {
  if constexpr (I < N) { f(IC<I>{}); static_for<I + 1, N>(f); }
}
DI unsigned pk2(float a, float b) {
  bf16x2_t v = {(__bf16)a, (__bf16)b};
  return __builtin_bit_cast(unsigned, v);
}
DI float bflo(unsigned w) { return __uint_as_float(w << 16); }
DI float bfhi(unsigned w) { return __uint_as_float(w & 0xffff0000u); }
DI s16x4 trd(lptr p) { return __builtin_bit_cast(s16x4, __builtin_amdgcn_ds_read_tr16_b64_v4i16((LAS s16x4*)p)); }
DI bf16x8 cat8(s16x4 lo, s16x4 hi) { return __builtin_shufflevector(lo, hi, 0, 1, 2, 3, 4, 5, 6, 7); }
DI bf16x8 ldfrag(lptr p) { return *(LAS bf16x8*)p; }
DI rsrc_t mkrs(const void* p, int bytes) { return __builtin_amdgcn_make_buffer_rsrc((void*)p, (short)0, bytes, 0x00020000); }
template <int LOFF, int GOFF> DI void dma(rsrc_t rs, unsigned voff, unsigned wl) {
  unsigned t;
  asm volatile("s_add_u32 m0, %3, %4\n\ts_mov_b32 %0, %5\n\tbuffer_load_dwordx4 %1, %2, %0 offen lds"
               : "=&s"(t) : "v"(voff), "s"(rs), "s"(wl), "i"(LOFF), "i"(GOFF) : "memory", "scc");
}
template <int LOFF, int GOFF> DI void dma_dev(rsrc_t rs, unsigned voff, unsigned wl) {
  unsigned t;
  asm volatile("s_add_u32 m0, %3, %4\n\ts_mov_b32 %0, %5\n\tbuffer_load_dwordx4 %1, %2, %0 offen sc1 lds"
               : "=&s"(t) : "v"(voff), "s"(rs), "s"(wl), "i"(LOFF), "i"(GOFF) : "memory", "scc");
}
DI void dma_dyn(rsrc_t rs, unsigned voff, unsigned soff, unsigned ldst) {
  asm volatile("s_mov_b32 m0, %3\n\ts_nop 0\n\tbuffer_load_dwordx4 %0, %1, %2 offen sc1 lds" ::"v"(voff), "s"(rs), "s"(soff), "s"(ldst) : "memory");
}
template <int LOFF> DI void touch(rsrc_t rs, unsigned voff, unsigned wl) {
  asm volatile("s_add_u32 m0, %2, %3\n\ts_nop 0\n\tbuffer_load_dword %0, %1, 0 offen lds" ::"v"(voff), "s"(rs), "s"(wl), "i"(LOFF) : "memory", "scc");
}
DI void st16_dev(rsrc_t rs, unsigned voff, u32x4 v) { __builtin_amdgcn_raw_buffer_store_b128(v, rs, voff, 0, 16); }
DI void st8_dev(rsrc_t rs, unsigned voff, u32x2 v) { __builtin_amdgcn_raw_buffer_store_b64(v, rs, voff, 0, 16); }
DI void st4_dev(rsrc_t rs, unsigned voff, unsigned v) { __builtin_amdgcn_raw_buffer_store_b32(v, rs, voff, 0, 16); }
DI u32x4 ld16_dev(rsrc_t rs, unsigned voff, int soff) { return __builtin_amdgcn_raw_buffer_load_b128(rs, voff, soff, 16); }
DI u32x2 ld8_dev(rsrc_t rs, unsigned voff) { return __builtin_amdgcn_raw_buffer_load_b64(rs, voff, 0, 16); }
DI unsigned ld4_dev(rsrc_t rs, unsigned voff) { return __builtin_amdgcn_raw_buffer_load_b32(rs, voff, 0, 16); }
template <int N> DI void wait_bar() { asm volatile("s_waitcnt vmcnt(%0) lgkmcnt(0)\n\ts_barrier" ::"i"(N) : "memory"); }
DI void lds_bar() { asm volatile("s_waitcnt lgkmcnt(0)\n\ts_barrier" ::: "memory"); }
DI float phi_f(float t) { return (t > 0.f ? t + 1.f : __builtin_amdgcn_exp2f(t * 1.4426950408889634f)) + 1e-6f; }
DI float xor16(float v) { auto r = __builtin_amdgcn_permlane16_swap(__float_as_uint(v), __float_as_uint(v), false, false); return __uint_as_float(r[0]) + __uint_as_float(r[1]); }
DI float xor32(float v) { auto r = __builtin_amdgcn_permlane32_swap(__float_as_uint(v), __float_as_uint(v), false, false); return __uint_as_float(r[0]) + __uint_as_float(r[1]); }
DI float sum_fq(float v) { return xor32(xor16(v)); }
DI void poll(rsrc_t rs, unsigned voff, unsigned t0, unsigned t1, unsigned need) {
#pragma unroll 1
  for (unsigned spin = 0; spin < (1u << 17); ++spin) {
    u32x2 f;
    asm volatile("buffer_load_dwordx2 %0, %1, %2, 0 offen sc1\n\ts_waitcnt vmcnt(0)" : "=v"(f) : "v"(voff), "s"(rs) : "memory");
    const unsigned d = f[0] - t0;
    if (__all(f[1] == t1 && d >= need && d < 16u)) break;
    __builtin_amdgcn_s_sleep(1);
  }
}

#define STAMP_DECL
#define STAMP(i) do {} while (0)

constexpr int KA_SLOT = 16384 + 10240;
constexpr int KA_KVO = 16384;
constexpr int ka_q(int kb) { return kb < 5 ? 4 * KA_SLOT + kb * 5120 : (kb - 5) * 5120; }
constexpr int KA_EX = KA_SLOT;
constexpr int KA_QC = 2 * KA_SLOT;
constexpr int KA_WO = 5 * KA_SLOT;
constexpr int KA_CS = KA_WO + 3072;
constexpr int KA_KVI = KA_CS + 8 * 320;
constexpr int KA_RSP = KA_KVI + 6144;
constexpr int KA_ZZ = KA_RSP + 160;
constexpr int KA_RS = KA_ZZ + 160;
constexpr int KA_DEAD = KA_RS + 320;
static_assert(KA_DEAD + 2048 <= 163840, "lds");

struct Tab { unsigned v[1024]; };
constexpr Tab make_tab() {
  Tab T{};
  for (int t = 0; t < 512; ++t) {
    const int w = t >> 6, ln = t & 63, fr = ln & 15, fq = ln >> 4;
    const int slot = w == 0 ? ln : w == 3 ? 64 + ln : w == 1 ? (fr < 2 ? 128 + fq * 2 + fr : -1) : w == 2 ? (fq == 0 ? 136 + fr : -1)
                   : w == 4 ? (fr == 8 ? 152 + fq : (ln < 8 ? 197 + ln : ln == 9 ? 205 : -1)) : w == 5 ? ((fr >= 4 && fr <= 8) ? 156 + fq * 5 + fr - 4 : -1)
                   : w == 6 ? (fq == 0 ? 176 + fr : -1) : ((fq == 0 && fr >= 4 && fr <= 8) ? 192 + fr - 4 : -1);
    int ce = 0, cd = 0;
    if (t < 128) { ce = (t & 15) + (t >> 6) * 16; cd = ((t >> 4) & 3) * 4 + (t >> 6) * 16; }
    else if (t < 136) { ce = 16 + (t & 1); cd = ((t - 128) >> 1) * 4; }
    else if (t < 152) { ce = t - 136; cd = 16; }
    else if (t < 156) { ce = 36; cd = (t - 152) * 4; }
    else if (t < 176) { const int i = t - 156, f = i / 5; ce = 32 + i - f * 5; cd = 16 + 4 * f; }
    else if (t < 192) { ce = t - 160; cd = 32; }
    else if (t < 197) { ce = 32 + t - 192; cd = 32; }
    int m = 0;
    for (int rr = 0; rr < 4; ++rr) m |= ((cd + rr >= 18) == (ce >= 18)) ? (1 << rr) : 0;
    int dst = KA_KVI + ce * 96 + cd * 2;
    if (ce == 36) { dst = KA_ZZ + cd * 4; m = 16; }
    if (t >= 197) { dst = KA_RS + (t < 206 ? (t - 197) * 32 : 0); m = t < 206 ? 32 : 64; }
    T.v[2 * t] = slot >= 0 ? (unsigned)slot * 16u : 0x40000000u;
    T.v[2 * t + 1] = (unsigned)dst | ((unsigned)m << 24);
  }
  return T;
}
static __device__ const Tab g_tab = make_tab();

constexpr int KB_B = 18 * 4096;
constexpr int KB_RM = KB_B + 18 * 4608;
constexpr int F_G = 8192, F_U = 8192 + 18432 + 16, F_STG = F_U + 18432;
constexpr int F_SSQ = 3 * F_STG;
constexpr int F_RS = F_SSQ + 8192;
constexpr int F_H = 163840 - 73728;
constexpr int f_wslot(int i) { return i == 0 ? 0 : i == 1 ? F_STG : i == 2 ? F_H + 36864 : F_H; }
constexpr int f_reg(int c) { return c < 8 ? (c & 3) : c == 8 ? 1 : c == 9 ? 2 : c == 10 ? 3 : c == 11 ? 1 : c == 12 ? 2 : c == 13 ? 0 : c == 14 ? 3 : c == 15 ? 1 : c == 16 ? 2 : 0; }

__global__ __launch_bounds__(512, 2) void mk_block(const u16* __restrict__ x, const u16* __restrict__ wqkv, const u16* __restrict__ wo,
                                                   const u16* __restrict__ wg, const u16* __restrict__ wu, const u16* __restrict__ wd,
                                                   char* __restrict__ ws, u16* __restrict__ out) {
  __shared__ __attribute__((aligned(16))) char smem[163840];
  STAMP_DECL; STAMP(0);
  const lptr L = (lptr)smem;
  const unsigned lds0 = (unsigned)(uintptr_t)L;
  const int tid = threadIdx.x, lane = tid & 63, w = __builtin_amdgcn_readfirstlane(tid >> 6);
  const int fr = lane & 15, fq = lane >> 4, q4 = fr >> 2, pp = lane & 3;
  const int xcd = blockIdx.x & 7, r = blockIdx.x >> 3;
  const unsigned wl1 = lds0 + w * 1024, wl2 = lds0 + w * 2048;
  const rsrc_t rW = mkrs(ws, WS_END), rX = mkrs(x, 2048 * 576 * 2);
  const rsrc_t rG = mkrs(wg, 576 * 4608 * 2), rU = mkrs(wu, 576 * 4608 * 2);
  const f32x4 z4 = {0.f, 0.f, 0.f, 0.f};
  unsigned tag0, tag1;
  const int gw = r * 8 + w;
  auto warm_gu = [&](auto lc, int row0, int nl) AI {
    const int li = gw * nl + min(lane, nl - 1), row = li / 9;
    const unsigned off = (unsigned)((row0 + row) * 9216 + (li - row * 9) * 128 + xcd * 1152);
    touch<decltype(lc)::v>(rG, off, lds0 + w * 256);
    touch<decltype(lc)::v>(rU, off, lds0 + w * 256);
  };

  {
    const rsrc_t rQ = mkrs(wqkv, 576 * 1728 * 2);
    const int tt = (xcd & 3) * 4 + (r & 3), p = (xcd >> 2) * 8 + (r >> 2);
    const int o8 = (p & 1) * 8, ot = (tt & 1) * 8;
    const int tok0 = tt * 128 + w * 16;
    const unsigned vx = (unsigned)((tok0 + (lane >> 3)) * 1152 + (((lane & 7) ^ (lane >> 3)) * 16));
    {
      const unsigned g = blockIdx.x * 8 + w;
      touch<KA_DEAD>(mkrs((const void*)(__builtin_amdgcn_s_getpc() & ~127ull), CODE_BYTES), (unsigned)(gw * 3 + min(lane, 2)) * 128, lds0 + w * 256);
      touch<KA_DEAD>(rX, lane < 9 ? (g * 9 + lane) * 128 : 0x7fffff00u, lds0 + w * 256);
      touch<KA_DEAD>(rQ, lane < 8 ? (g * 8 + lane) * 128 : 0x7fffff00u, lds0 + w * 256);
    }
    dma<0, 0>(rX, vx, wl2);
    dma<1024, 8 * 1152>(rX, vx, wl2);
    auto mkv = [&](int pi) AI {
      const int c = pi * 64 + lane, row = c / 10, cc = c - row * 10, rho = row & 31;
      const int k = (row & 32) + ((rho >> 2) & 3) * 8 + (rho >> 4) * 4 + (rho & 3);
      return (unsigned)(k * 3456 + (cc >= 5 ? 1152 - 80 : 0) + cc * 16 + 1152 + 72 * p - o8);
    };
    const unsigned vkv0 = mkv(w), vkv8 = mkv(8 + (w & 1));
    const bool xtra = w >= 6;
    const unsigned wlx = lds0 + (w & 1) * 1024;
    auto piece = [&](auto kbc, int i) AI {
      constexpr int kb = decltype(kbc)::v, so = (kb % 5) * KA_SLOT;
      if ((ABL == 2 || ABL == 9) && kb >= 3) return;
      if (i == 0) dma<so, kb * 128>(rX, vx, wl2);
      if (i == 1) dma<so + 1024, kb * 128 + 8 * 1152>(rX, vx, wl2);
      if (i == 2) dma<so + KA_KVO, kb * 64 * 3456>(rQ, vkv0, wl1);
      if (i == 3) { if (xtra) dma<so + KA_KVO + 8192, kb * 64 * 3456>(rQ, vkv8, wlx); }
    };
    piece(IC<0>{}, 2); piece(IC<0>{}, 3);
    piece(IC<1>{}, 0); piece(IC<1>{}, 1); piece(IC<1>{}, 2); piece(IC<1>{}, 3);
    u32x2 ep, tab;
    asm volatile("buffer_load_dwordx2 %0, off, %1, 0 sc1" : "=v"(ep) : "s"(rW) : "memory");
    asm volatile("global_load_dwordx2 %0, %1, %2" : "=v"(tab) : "v"(tid * 8), "s"(g_tab.v) : "memory");
    if (w < 3) {
      const int c = min(tid, 179), rw = c / 5;
      dma<KA_WO, 0>(mkrs(wo, 576 * 576 * 2), (unsigned)((36 * p + rw) * 1152 + 72 * tt - ot + (c - rw * 5) * 16), wl1);
    }
    piece(IC<2>{}, 0); piece(IC<2>{}, 1); piece(IC<2>{}, 2); piece(IC<2>{}, 3);
    if (tid < 384) *(LAS u32x4*)(L + KA_KVI + tid * 16) = (u32x4){0u, 0u, 0u, 0u};
    unsigned vq;
    {
      const int c = (w < 5 ? w : 0) * 64 + lane, row = c / 5, ch = c - row * 5;
      const int rho = row & 31, rr = rho & 15;
      vq = (unsigned)(((row & 32) + 8 * (2 * (rr & 1) + (rr >> 3)) + 4 * (rho >> 4) + ((rr >> 1) & 3)) * 3456 + ch * 16 + 72 * p - o8);
    }
    lptr bk[3], bx[3], ba0[3], ba1[3];
    bk[0] = L + KA_KVO + (4 * fq + q4) * 160 + o8 + 8 * pp;
    bx[0] = L + KA_KVO + (4 * fq + q4) * 160 + 80 * (pp & 1) + o8 + 64;
    ba0[0] = L + w * 2048 + fr * 128 + (fq ^ (fr & 7)) * 16;
    ba1[0] = L + w * 2048 + fr * 128 + ((4 + fq) ^ (fr & 7)) * 16;
#pragma unroll
    for (int i = 1; i < 3; ++i) {
      bk[i] = bk[0] + 2 * i * KA_SLOT; bx[i] = bx[0] + 2 * i * KA_SLOT; ba0[i] = ba0[0] + 2 * i * KA_SLOT; ba1[i] = ba1[0] + 2 * i * KA_SLOT;
      PIN(bk[i]); PIN(bx[i]); PIN(ba0[i]); PIN(ba1[i]);
    }
    f32x4 acc[5], cs[5], gram = z4, sx = z4;
#pragma unroll
    for (int i = 0; i < 5; ++i) acc[i] = cs[i] = z4;
    bf16x8 ones = {0x3F80, 0x3F80, 0x3F80, 0x3F80, 0x3F80, 0x3F80, 0x3F80, 0x3F80};
    PIN(ones);
    bf16x8 a[18], B[2][5];
    auto readA = [&](int kb) AI {
      const int sl = kb % 5;
      a[2 * kb] = ldfrag(ba0[sl >> 1] + (sl & 1) * KA_SLOT);
      a[2 * kb + 1] = ldfrag(ba1[sl >> 1] + (sl & 1) * KA_SLOT);
    };
    auto readB = [&](int s, int i) AI {
      const int sl = (s >> 1) % 5, so = (sl & 1) * KA_SLOT + (s & 1) * 5120;
      if ((ABL == 3 || ABL == 9) && s >= 2) return;
      if (i < 4) {
        const int off = so + (i >> 1) * 80 + 32 * (i & 1);
        B[s & 1][i] = cat8(trd(bk[sl >> 1] + off), trd(bk[sl >> 1] + off + 2560));
      } else B[s & 1][4] = cat8(trd(bx[sl >> 1] + so), trd(bx[sl >> 1] + so + 2560));
    };
    auto issue_q = [&](auto kc) AI {
      constexpr int k = decltype(kc)::v;
      dma<ka_q(k), k * 64 * 3456>(rQ, vq, wl1);
    };
    auto slot = [&](auto kbc, auto slc) AI {
      constexpr int KB = decltype(kbc)::v, sl = decltype(slc)::v;
      if constexpr (KB <= 0) {
        constexpr int b = KB < 0 ? 3 : (sl < 3 ? 4 : 5);
        piece(IC<b>{}, sl % 3);
        if constexpr (sl % 3 == 2) piece(IC<b>{}, 3);
      }
      if constexpr (KB >= 1 && KB <= 3 && sl < 4) piece(IC<KB + 5>{}, sl);
      if constexpr (KB == 4 && sl < 5) { if (w < 5) issue_q(IC<sl>{}); }
      if constexpr (KB == 5 && sl < 4) { if (w < 5) issue_q(IC<5 + sl>{}); }
    };
    auto step = [&](auto sc, auto kbc, auto s0c) AI {
      constexpr int s = decltype(sc)::v, s0 = decltype(s0c)::v;
      constexpr bool nx = s < 17;
      acc[0] = MFMA16(B[s & 1][0], a[s], acc[0]); acc[1] = MFMA16(B[s & 1][1], a[s], acc[1]); SBAR();
      if constexpr (nx && (s & 1)) readA((s + 1) >> 1);
      if constexpr (nx) { readB(s + 1, 0); readB(s + 1, 1); }
      slot(kbc, IC<s0>{}); SBAR();
      acc[2] = MFMA16(B[s & 1][2], a[s], acc[2]); acc[3] = MFMA16(B[s & 1][3], a[s], acc[3]); SBAR();
      if constexpr (nx) { readB(s + 1, 2); readB(s + 1, 3); }
      slot(kbc, IC<s0 + 1>{}); SBAR();
      acc[4] = MFMA16(B[s & 1][4], a[s], acc[4]); gram = MFMA16(a[s], a[s], gram); sx = MFMA16(ones, a[s], sx); SBAR();
      if constexpr (nx) readB(s + 1, 4);
      slot(kbc, IC<s0 + 2>{}); SBAR();
      if (s >= 15 || (w & 1) == (s & 1)) {
#pragma unroll
        for (int i = 0; i < 5; ++i) cs[i] = MFMA16(B[s & 1][i], ones, cs[i]);
      }
      SBAR();
    };
    STAMP(1);
    wait_bar<8>();
    STAMP(2);
    readA(0);
#pragma unroll
    for (int i = 0; i < 5; ++i) readB(0, i);
    f32x4 csp[5];
    static_for<0, 9>([&](auto ic) AI {
      constexpr int KB = decltype(ic)::v;
      step(IC<2 * KB>{}, IC<KB - 1>{}, IC<3>{});
      if constexpr (KB == 2) {
        if (tid < 36) {
          float s = 0.f;
#pragma unroll
          for (int i = 0; i < 9; ++i) {
            const u32x2 v = *(LAS u32x2*)(L + KA_WO + tid * 80 + ot + i * 8);
            s += (bflo(v[0]) + bfhi(v[0])) + (bflo(v[1]) + bfhi(v[1]));
          }
          *(LAS float*)(L + KA_RSP + tid * 4) = s;
        }
      }
      if constexpr (KB == 7) {
        if (fr == 0) {
#pragma unroll
          for (int i = 0; i < 5; ++i) *(LAS f32x4*)(L + KA_CS + w * 320 + (i * 4 + fq) * 16) = cs[i];
        }
      }
      if constexpr (KB == 0) wait_bar<8>();
      else if constexpr (KB <= 4) wait_bar<9>();
      else if constexpr (KB == 5) { if (w < 5) wait_bar<11>(); else wait_bar<6>(); }
      else if constexpr (KB == 6) { if (w < 5) wait_bar<12>(); else wait_bar<3>(); }
      else if constexpr (KB == 7) { if (w < 5) wait_bar<9>(); else wait_bar<0>(); }
      if constexpr (KB == 7) {
#pragma unroll
        for (int i = 0; i < 5; ++i) csp[i] = *(LAS f32x4*)(L + KA_CS + (w ^ 1) * 320 + (i * 4 + fq) * 16);
      }
      step(IC<2 * KB + 1>{}, ic, IC<0>{});
    });
    STAMP(3);
    float meanT, rstdT;
    {
      const int rr = fr & 3;
      float g = rr == 0 ? gram[0] : rr == 1 ? gram[1] : rr == 2 ? gram[2] : gram[3];
      g = sum_fq(q4 == fq ? g : 0.f);
      meanT = sx[0] * (1.f / 576.f);
      rstdT = __builtin_amdgcn_rsqf(fmaxf(g * (1.f / 576.f) - meanT * meanT, 0.f) + 1e-5f);
      const lptr exw = L + KA_EX + (w * 16 + fr) * 160 + fq * 8;
#pragma unroll
      for (int i = 0; i < 5; ++i) {
        float v[4];
#pragma unroll
        for (int k = 0; k < 4; ++k) {
          const float t = (acc[i][k] - meanT * (cs[i][k] + csp[i][k])) * rstdT;
          if (i < 2) v[k] = phi_f(t);
          else if (i < 4) v[k] = t;
          else { const float ph = phi_f(t); v[k] = fq == 0 ? ph : (fq == 1 ? t : 1.0f); }
        }
        *(LAS u32x2*)(exw + i * 32) = (u32x2){pk2(v[0], v[1]), pk2(v[2], v[3])};
      }
    }
    STAMP(4);
    asm volatile("s_waitcnt vmcnt(0) lgkmcnt(0)\n\ts_barrier" : "+v"(ep), "+v"(tab)::"memory");
    const unsigned recb = WS_REC + p * 16 * REC;
    {
      const int sa = (0x44101100 >> (4 * w)) & 7, sb = (0x43443232 >> (4 * w)) & 7;
      const lptr ex = L + KA_EX + (4 * fq + q4) * 160 + pp * 8;
      const lptr exa = ex + sa * 32, exb = ex + sb * 32;
      bf16x8 fa[4], fb[4];
#pragma unroll
      for (int k = 0; k < 4; ++k) {
        fa[k] = cat8(trd(exa + k * 5120), trd(exa + k * 5120 + 2560));
        fb[k] = cat8(trd(exb + k * 5120), trd(exb + k * 5120 + 2560));
      }
      const f32x4 rsp = *(LAS f32x4*)(L + KA_RSP + min(lane, 8) * 16);
      f32x4 T = z4;
#pragma unroll
      for (int k = 0; k < 4; ++k) T = MFMA16(fa[k], fb[k], T);
      if (tab[0] >= 197 * 16) T = rsp;
      st16_dev(rW, recb + tt * REC + tab[0], __builtin_bit_cast(u32x4, T));
    }
    STAMP(5);
    tag0 = __builtin_amdgcn_readfirstlane((ep[0] * 0x9E3779B1u + 0x7F4A7C15u + ep[1]) & ~15u);
    tag1 = __builtin_amdgcn_readfirstlane((ep[1] ^ (ep[0] >> 7)) * 0x85EBCA6Bu + 0x165667B1u);
    if (tag1 == (unsigned)__builtin_amdgcn_readfirstlane(ep[1])) tag1 ^= 0x5bd1e995u;
    f32x4 qa[3];
    {
#pragma unroll
      for (int i = 0; i < 3; ++i) qa[i] = z4;
      lptr bq[2], bqp[2];
      bq[0] = L + (2 * (4 * (fq & 1) + q4) + (fq >> 1)) * 80 + o8 + 64;
      bqp[0] = bq[0] - 64 + 8 * pp;
      bq[1] = bq[0] + 4 * KA_SLOT; bqp[1] = bqp[0] + 4 * KA_SLOT;
      PIN(bq[1]); PIN(bqp[1]);
      bf16x8 b[3][3];
      auto rq = [&](int s) AI {
        const int h = s < 10 ? 1 : 0, o = (s < 10 ? s : s - 10) * 2560;
        b[s % 3][0] = cat8(trd(bqp[h] + o), trd(bqp[h] + o + 1280));
        b[s % 3][1] = cat8(trd(bqp[h] + o + 32), trd(bqp[h] + o + 1280 + 32));
        b[s % 3][2] = cat8(trd(bq[h] + o), trd(bq[h] + o + 1280));
      };
      if (w >= 4) {
        f32x4 qc[3] = {z4, z4, z4};
        const int j = w & 3;
#pragma unroll
        for (int i = 0; i < 3; ++i) {
          const int kb = j + 4 * i, o = kb < 5 ? 4 * KA_SLOT + kb * 5120 : (kb - 5) * 5120;
          if (i < 2 || j == 0) {
#pragma unroll
            for (int h = 0; h < 2; ++h) {
              const lptr q0 = bqp[0] + o + h * 2560, q1 = bq[0] + o + h * 2560;
              qc[0] = MFMA16(cat8(trd(q0), trd(q0 + 1280)), ones, qc[0]);
              qc[1] = MFMA16(cat8(trd(q0 + 32), trd(q0 + 1280 + 32)), ones, qc[1]);
              qc[2] = MFMA16(cat8(trd(q1), trd(q1 + 1280)), ones, qc[2]);
            }
          }
        }
        if (fr == 0) {
#pragma unroll
          for (int i = 0; i < 3; ++i) *(LAS f32x4*)(L + KA_QC + ((i * 4 + fq) * 4 + j) * 16) = qc[i];
        }
        asm volatile("s_waitcnt vmcnt(0) lgkmcnt(0)\n\ts_barrier" ::: "memory");
        const unsigned off = (unsigned)((r * 4 + j) * 9216 + min(lane, 8) * 128 + xcd * 1152);
        touch<KA_DEAD>(rG, off, lds0 + w * 256);
        touch<KA_DEAD>(rU, off, lds0 + w * 256);
      }
      rq(0); rq(1);
      static_for<0, 18>([&](auto ic) AI {
        constexpr int s = decltype(ic)::v;
        if constexpr (s < 16) rq(s + 2);
#pragma unroll
        for (int i = 0; i < 3; ++i) qa[i] = MFMA16(b[s % 3][i], a[s], qa[i]);
        SBAR();
        if constexpr (s == PUBSTEP) {
          if (w < 4) {
            asm volatile("s_waitcnt vmcnt(0)\n\ts_barrier" ::: "memory");
            if (tid == 0) st8_dev(rW, WS_F1 + (p * 16 + tt) * 64, (u32x2){tag0, tag1});
            STAMP(6);
          }
        }
      });
      if (p == 0 && fq == 0) st4_dev(rW, WS_MEAN1 + (tok0 + fr) * 4, __float_as_uint(meanT));
      float pq[3][4];
#pragma unroll
      for (int i = 0; i < 3; ++i) {
        const lptr qp = L + KA_QC + (i * 4 + fq) * 64;
        const f32x4 cq = (*(LAS f32x4*)qp + *(LAS f32x4*)(qp + 16)) + (*(LAS f32x4*)(qp + 32) + *(LAS f32x4*)(qp + 48));
#pragma unroll
        for (int k = 0; k < 4; ++k) { pq[i][k] = phi_f((qa[i][k] - meanT * cq[k]) * rstdT); PIN(pq[i][k]); }
      }
      STAMP(7);
      const int ct = w < 3 ? w : w < 6 ? w - 3 : 0;
      lptr wa = L + KA_KVI + (4 * fq + q4) * 96 + (w < 3 ? 0 : 64) + pp * 8;
      lptr wb[4];
#pragma unroll
      for (int tq = 0; tq < 4; ++tq) {
        const int ec = tq < 2 ? 16 * tq + 4 * fq + q4 : min(16 * tq + 4 * fq + q4, 35);
        wb[tq] = (ct == 2 && pp > 0) ? L + KA_RS + ec * 8 : L + KA_WO + ec * 80 + ot + ct * 32 + pp * 8;
        PIN(wb[tq]);
      }
      unsigned wst;
      {
        const int c36 = 16 * ct + fr, c72 = (tt & 1) * 36 + c36, sub = c72 >> 4, frc = c72 & 15;
        const int ks = w < 3 ? p : 16 + (p >> 3), fqq = w < 3 ? fq : (p & 3), hb = w < 3 ? 0 : ((p >> 2) & 1) * 8;
        wst = WS_WEF + ((tt >> 1) * 18 + ks) * 4608 + (sub < 4 ? sub * 1024 + (frc + 16 * fqq) * 16 : 4096 + (frc + 8 * fqq) * 16) + hb;
        if (c36 == 36) wst = WS_RMF + (tt >> 1) * 2048 + (ks * 4 + fqq) * 16 + hb;
        if (c36 > 36 || (c36 == 36 && (tt & 1)) || (w >= 3 && fq != 0) || w >= 6) wst = 0x40000000u;
      }
      PIN(wa); PIN(wst);
      const unsigned afb = WS_AF + (tok0 >> 6) * 18 * 4096 + ((tok0 >> 4) & 3) * 1024;
      u32x4 v[16];
      if (w < 4) {
        STAMP(8);
        poll(rW, WS_F1 + (p * 16 + fr) * 64, tag0, tag1, 0);
        STAMP(9);
        const unsigned vo = recb + min(tid, 205) * 16;
#pragma unroll
        for (int t = 0; t < 16; ++t) v[t] = ld16_dev(rW, vo, t * REC);
      }
      if (w < 4) {
#define BC(i) __builtin_bit_cast(f32x4, v[i])
        const f32x4 s = (((BC(0) + BC(1)) + (BC(2) + BC(3))) + ((BC(4) + BC(5)) + (BC(6) + BC(7)))) +
                        (((BC(8) + BC(9)) + (BC(10) + BC(11))) + ((BC(12) + BC(13)) + (BC(14) + BC(15))));
#undef BC
        const unsigned cm = tab[1] >> 24;
        const lptr cd = L + (tab[1] & 0xffffff);
        if (cm < 16) {
          *(LAS u32x2*)cd = (u32x2){pk2((cm & 1) ? s[0] : 0.f, (cm & 2) ? s[1] : 0.f), pk2((cm & 4) ? s[2] : 0.f, (cm & 8) ? s[3] : 0.f)};
        } else if (cm == 16) {
          *(LAS f32x4*)cd = s;
        } else if (cm == 32) {
          const f32x4 t = s * (1.f / 576.f);
          *(LAS u32x4*)cd = (u32x4){pk2(t[0], 0.f), 0u, pk2(t[1], 0.f), 0u};
          *(LAS u32x4*)(cd + 16) = (u32x4){pk2(t[2], 0.f), 0u, pk2(t[3], 0.f), 0u};
        }
      }
      lds_bar();
      STAMP(10);
      {
        bf16x8 fa[2][2], fb[2];
#pragma unroll
        for (int k = 0; k < 2; ++k) {
          fa[0][k] = cat8(trd(wa + k * 3072), trd(wa + k * 3072 + 1536));
          fa[1][k] = cat8(trd(wa + k * 3072 + 32), trd(wa + k * 3072 + 1536 + 32));
          fb[k] = cat8(trd(wb[2 * k]), trd(wb[2 * k + 1]));
        }
        f32x4 zz[3];
#pragma unroll
        for (int i = 0; i < 3; ++i) zz[i] = *(LAS f32x4*)(L + KA_ZZ + (i < 2 ? 16 * i + 4 * fq : 32) * 4);
        const f32x4 T0 = MFMA16(fa[0][1], fb[1], MFMA16(fa[0][0], fb[0], z4));
        const f32x4 T1 = MFMA16(fa[1][1], fb[1], MFMA16(fa[1][0], fb[0], z4));
        float d0 = (pq[0][0] * zz[0][0] + pq[0][1] * zz[0][1]) + (pq[0][2] * zz[0][2] + pq[0][3] * zz[0][3]);
        const float lo2 = pq[1][0] * zz[1][0] + pq[1][1] * zz[1][1];
        float d1 = pq[1][2] * zz[1][2] + pq[1][3] * zz[1][3];
        const float x2 = (pq[2][0] * zz[2][0] + pq[2][1] * zz[2][1]) + (pq[2][2] * zz[2][2] + pq[2][3] * zz[2][3]);
        d0 += fq == 0 ? lo2 : 0.f;
        d1 += fq == 0 ? x2 : lo2;
        d0 = sum_fq(d0);
        d1 = sum_fq(d1);
        const float i0 = __builtin_amdgcn_rcpf(d0 + 1e-6f), i1 = __builtin_amdgcn_rcpf(d1 + 1e-6f), il = fq == 0 ? i0 : i1;
        st16_dev(rW, afb + p * 4096 + lane * 16,
                 (u32x4){pk2(pq[0][0] * i0, pq[0][1] * i0), pk2(pq[0][2] * i0, pq[0][3] * i0), pk2(pq[1][0] * il, pq[1][1] * il), pk2(pq[1][2] * i1, pq[1][3] * i1)});
        if (fq == 0)
          st8_dev(rW, afb + (16 + (p >> 3)) * 4096 + (fr + 16 * (p & 3)) * 16 + ((p >> 2) & 1) * 8,
                  (u32x2){pk2(pq[2][0] * i1, pq[2][1] * i1), pk2(pq[2][2] * i1, pq[2][3] * i1)});
        const u32x4 wv = {pk2(T0[0], T0[1]), pk2(T0[2], T0[3]), pk2(T1[0], T1[1]), pk2(T1[2], T1[3])};
        if (w < 3) st16_dev(rW, wst, wv); else st8_dev(rW, wst, (u32x2){wv[0], wv[1]});
      }
    }
    STAMP(11);
    asm volatile("s_waitcnt vmcnt(0) lgkmcnt(0)\n\ts_barrier" ::: "memory");
    if (tid == 0) st8_dev(rW, WS_F2 + (p * 16 + tt) * 64, (u32x2){tag0, tag1});
    STAMP(12);
  }

  {
    const int t64 = (xcd & 3) * 8 + (r & 7), j = (xcd >> 2) * 4 + (r >> 3);
    const int msub = w & 3, kh = w >> 2;
    const int tok = t64 * 64 + msub * 16 + fr;
    u32x2 xv[3];
    {
      const char* xr = (const char*)x + (size_t)tok * 1152 + 144 * j;
      xv[0] = *(const u32x2*)(xr + kh * 64 + 8 * fq);
      xv[1] = *(const u32x2*)(xr + kh * 64 + 32 + 8 * fq);
      xv[2] = *(const u32x2*)(xr + 128 + 8 * (fq & 1));
    }
    if (w == 0) {
      const int i = lane < 48 ? lane : 0;
      poll(rW, WS_F2 + (i < 16 ? i * 16 + (t64 >> 1) : ((i - 16) >> 1) * 16 + 2 * j + (i & 1)) * 64, tag0, tag1, 0);
      if (blockIdx.x == 0 && lane == 0) st8_dev(rW, WS_EPOCH, (u32x2){tag0, tag1});
    }
    asm volatile("s_barrier" ::: "memory");
    STAMP(13);
    const unsigned va = WS_AF + t64 * 18 * 4096 + tid * 16, vb = WS_WEF + j * 18 * 4608 + tid * 16;
    if (w == 3 || w == 4) dma_dev<KB_RM - 3 * 1024, 0>(rW, WS_RMF + j * 2048 + tid * 16 - 3 * 1024, wl1);
    const unsigned m1 = ld4_dev(rW, WS_MEAN1 + tok * 4);
    static_for<0, 3>([&](auto gc) AI {
      constexpr int g = decltype(gc)::v;
      dma_dev<g * 24576, g * 24576>(rW, va, wl1);
      dma_dev<KB_B + g * 27648, g * 27648>(rW, vb, wl1);
      dma_dev<g * 24576 + 8192, g * 24576 + 8192>(rW, va, wl1);
      dma_dev<KB_B + g * 27648 + 8192, g * 27648 + 8192>(rW, vb, wl1);
      dma_dev<g * 24576 + 16384, g * 24576 + 16384>(rW, va, wl1);
      dma_dev<KB_B + g * 27648 + 16384, g * 27648 + 16384>(rW, vb, wl1);
      if (w < 3) dma_dev<KB_B + g * 27648 + 24576, g * 27648 + 24576>(rW, vb, wl1);
    });
    warm_gu(IC<161792>{}, 128, 8);
    SBAR();
    f32x4 acc[5];
#pragma unroll
    for (int i = 0; i < 5; ++i) acc[i] = z4;
    const lptr pa = L + kh * 4096 + msub * 1024 + lane * 16;
    lptr pb[2];
    pb[0] = L + KB_B + kh * 4608 + lane * 16;
    pb[1] = pb[0] + 5 * 9216;
    PIN(pb[1]);
    lptr p4 = fr < 8 ? L + KB_B + kh * 4608 + 4096 + (fr + 8 * fq) * 16 : L + KB_RM + (kh * 4 + fq) * 16;
    const int p4s = fr < 8 ? 9216 : 128;
    static_for<0, 3>([&](auto gc) AI {
      constexpr int g = decltype(gc)::v;
      wait_bar<6 * (2 - g) + 2>();
      if constexpr (g == 0) STAMP(14);
      bf16x8 av[3], bv[3][5];
#pragma unroll
      for (int k = 0; k < 3; ++k) {
        const int kb = 3 * g + k;
        av[k] = ldfrag(pa + kb * 8192);
#pragma unroll
        for (int i = 0; i < 4; ++i) bv[k][i] = ldfrag((kb < 5 ? pb[0] + kb * 9216 : pb[1] + (kb - 5) * 9216) + i * 1024);
        bv[k][4] = ldfrag(p4);
        p4 += p4s;
      }
#pragma unroll
      for (int k = 0; k < 3; ++k)
#pragma unroll
        for (int i = 0; i < 5; ++i) acc[i] = MFMA16(bv[k][i], av[k], acc[i]);
    });
    STAMP(15);
    lds_bar();
    {
      const lptr xs = L + (msub * 2 + kh) * 3072 + lane * 16;
      *(LAS f32x4*)(xs) = kh == 0 ? acc[2] : acc[0];
      *(LAS f32x4*)(xs + 1024) = kh == 0 ? acc[3] : acc[1];
      *(LAS f32x4*)(xs + 2048) = acc[4];
    }
    lds_bar();
    f32x4 o[3];
    {
      const lptr xs = L + (msub * 2 + (1 - kh)) * 3072 + lane * 16;
      o[0] = (kh == 0 ? acc[0] : acc[2]) + *(LAS f32x4*)(xs);
      o[1] = (kh == 0 ? acc[1] : acc[3]) + *(LAS f32x4*)(xs + 1024);
      o[2] = acc[4] + *(LAS f32x4*)(xs + 2048);
    }
    const float mean2 = __uint_as_float(m1) + sum_fq(fq == 2 ? o[2][0] : 0.f);
    u32x2 pk[3];
    float ssq = 0.f;
#pragma unroll
    for (int i = 0; i < 3; ++i) {
      pk[i] = (u32x2){pk2(bflo(xv[i][0]) + o[i][0] - mean2, bfhi(xv[i][0]) + o[i][1] - mean2),
                      pk2(bflo(xv[i][1]) + o[i][2] - mean2, bfhi(xv[i][1]) + o[i][3] - mean2)};
      const float a0 = bflo(pk[i][0]), a1 = bfhi(pk[i][0]), a2 = bflo(pk[i][1]), a3 = bfhi(pk[i][1]);
      const float q = (a0 * a0 + a1 * a1) + (a2 * a2 + a3 * a3);
      ssq += (i < 2 || (kh == 0 && fq < 2)) ? q : 0.f;
    }
    ssq = sum_fq(ssq);
    const unsigned hb = WS_HCF + ((tok >> 7) * 18 * 8 + ((tok >> 4) & 7)) * 1024;
    st16_dev(rW, hb + (2 * j + kh) * 8192 + lane * 16, (u32x4){pk[0][0], pk[0][1], pk[1][0], pk[1][1]});
    if (kh == 0 && fq < 2) st8_dev(rW, hb + (16 + (j >> 2)) * 8192 + (fr + 16 * (j & 3)) * 16 + fq * 8, pk[2]);
    if (fq == 0) st4_dev(rW, WS_SSQ + (2 * j + kh) * 8192 + tok * 4, __float_as_uint(ssq));
    if (j == 0 && kh == 0 && fq == 1) st4_dev(rW, WS_MEAN2 + tok * 4, __float_as_uint(mean2));
    STAMP(16);
    asm volatile("s_waitcnt vmcnt(0) lgkmcnt(0)\n\ts_barrier" ::: "memory");
    if (tid == 0) st8_dev(rW, WS_F3 + (t64 * 8 + j) * 64, (u32x2){tag0, tag1});
    STAMP(17);
  }

  const int fs = xcd * 2 + (r & 1), mh = r >> 1;
  const int hh = fs & 1, t64o = mh * 2 + hh;
  {
    const rsrc_t rD = mkrs(wd, 576 * 4608 * 2);
    const int wm = w & 1, wn = w >> 1;
    const bool lo = w < 4;
    unsigned vb0, vb1, vbx;
    auto mkb = [&](int pi, bool left) AI {
      const int c = pi * 64 + lane, rho = c / 36, cs = c - rho * 36;
      const int row = left ? 72 * ((rho >> 2) & 3) + 64 + 4 * (rho >> 4) + (rho & 3) : rho;
      return (unsigned)(row * 9216 + ((((cs >> 1) ^ ((rho >> 2) & 1)) << 1) + (cs & 1)) * 16 + fs * 576);
    };
    vb0 = mkb(w, false); vb1 = mkb(w + 8, false); vbx = mkb(16 + (w & 1), false);
    const rsrc_t rXt = w < 2 ? rG : rU;
    const unsigned wlx = lds0 + (w < 2 ? F_G : F_U) + (16 + (w & 1)) * 1024;
    const unsigned vA = WS_HCF + mh * 18 * 8192 + tid * 16;
    const unsigned vd = (unsigned)((fs * 288 + w * 8 + (lane >> 3)) * 1152 + (((((lane & 7) >> 1) ^ (fq & 3)) << 1) + (lane & 1)) * 16);
    const unsigned vd2 = vd + (hh ? -288 * 1152 : 288 * 1152);
    auto p1 = [&](auto sc, int i) AI {
      constexpr int s = decltype(sc)::v, so = (s % 3) * F_STG;
      constexpr int go = (s < 16 ? 72 * (s >> 1) + 32 * (s & 1) : 288 * (s - 16)) * 9216;
      if (i == 0) dma<so + F_G, go>(rG, vb0, wl1);
      if (i == 1) dma<so + F_U, go>(rU, vb0, wl1);
      if (i == 2) dma_dev<so, s * 8192>(rW, vA, wl1);
      if (i == 3) dma<so + F_G + 8192, go>(rG, vb1, wl1);
      if (i == 4) dma<so + F_U + 8192, go>(rU, vb1, wl1);
      if (i == 5) dma<so, go>(rXt, vbx, wlx);
    };
    auto p2 = [&](auto cc, auto ic) AI {
      constexpr int c = decltype(cc)::v, i = decltype(ic)::v;
      dma<f_wslot(f_reg(c)) + i * 8192, (c % 9) * 128 + i * 64 * 1152>(rD, c < 9 ? vd : vd2, wl1);
    };
    auto slot = [&](auto stc, auto slc) AI {
      constexpr int st = decltype(stc)::v, sl = decltype(slc)::v;
      if constexpr (st < 18) {
        constexpr int pl = sl == 0 ? 0 : sl == 2 ? 1 : sl == 3 ? 2 : sl == 5 ? 3 : sl == 6 ? 4 : sl == 8 ? 5 : -1;
        constexpr int ph = sl == 1 ? 0 : sl == 2 ? 1 : sl == 4 ? 2 : sl == 5 ? 3 : sl == 7 ? 4 : -1;
        if constexpr (pl >= 0 && pl == ph) p1(stc, pl);
        else if constexpr (pl >= 0) { if (lo) p1(stc, pl); }
        else if constexpr (ph >= 0) { if (!lo) p1(stc, ph); }
      } else if constexpr (st < 21) {
        if constexpr ((sl & 1) == 0) { if (lo) p2(IC<st - 18>{}, IC<(sl >> 1)>{}); }
        else { if (!lo) p2(IC<st - 18>{}, IC<(sl >> 1)>{}); }
      }
    };
    auto wts = [&]() AI {
      static_for<0, 3>([&](auto sc) AI {
        p1(sc, 0); p1(sc, 1); p1(sc, 3); p1(sc, 4);
        if (lo) p1(sc, 5);
      });
    };
    if (w == (mh & 7)) { poll(rW, WS_F3 + (mh * 16 + fr) * 64, tag0, tag1, 0); asm volatile("s_barrier" ::: "memory"); wts(); }
    else { wts(); asm volatile("s_barrier" ::: "memory"); }
    STAMP(18);
    dma_dev<F_SSQ, 0>(rW, (unsigned)(WS_SSQ + (2 * w + (lane >> 5)) * 8192 + mh * 512 + (lane & 31) * 16), wl1);
    p1(IC<0>{}, 2); p1(IC<1>{}, 2); p1(IC<2>{}, 2);
    const int gb = fq & 1;
    const int rowb = (4 * fq + q4) * 576;
    lptr pE[3], pO[3], pX[3], pA[2];
    pE[0] = L + F_G + rowb + 128 * wn + 32 * gb + 8 * pp;
    pO[0] = L + F_G + rowb + 128 * wn - 32 * gb + 8 * pp;
    pX[0] = L + rowb + (pp < 2 ? F_G : F_U) + ((16 + (wn >> 1)) ^ gb) * 32 + 16 * (wn & 1) + 8 * (pp & 1);
    pA[0] = L + (wm * 4) * 1024 + lane * 16;
    pE[1] = pE[0] + F_STG; pO[1] = pO[0] + F_STG; pE[2] = pE[0] + 2 * F_STG; pO[2] = pO[0] + 2 * F_STG;
    pX[1] = pX[0]; pX[2] = pX[0] + 2 * F_STG; pA[1] = pA[0] + 2 * F_STG;
    PIN(pE[1]); PIN(pO[1]); PIN(pE[2]); PIN(pO[2]); PIN(pX[2]); PIN(pA[1]);

    f32x4 acc[9][4];
    bf16x8 A[2][4], B[9];
    auto rdA = [&](int s, int m) AI { A[s & 1][m] = ldfrag((s % 3 == 2 ? pA[1] : pA[0] + (s % 3) * F_STG) + m * 1024); };
    auto rdB = [&](int s, int n) AI {
      const int b = s % 3;
      if (n < 8) {
        const int nn = n >> 1, off = ((n & 1) ? F_U - F_G : 0) + 32 * nn;
        const lptr q = (nn & 1) ? pO[b] : pE[b];
        B[n] = cat8(trd(q + off), trd(q + off + 16 * 576));
      } else {
        const lptr q = pX[b] + (b == 1 ? F_STG : 0);
        B[n] = cat8(trd(q), trd(q + 16 * 576));
      }
    };
    auto grh = [&](int s, int n, int h) AI {
#pragma unroll
      for (int m = 2 * h; m < 2 * h + 2; ++m) {
        if (s == 0) MFMA_NEW(acc[n][m], B[n], A[s & 1][m]); else MFMA_ACC(acc[n][m], B[n], A[s & 1][m]);
      }
      SBAR();
    };
    wait_bar<2>();
    STAMP(19);
#pragma unroll
    for (int m = 0; m < 4; ++m) rdA(0, m);
#pragma unroll
    for (int n = 0; n < 8; ++n) rdB(0, n);
    if (tid < 128) {
      float s = 0.f;
#pragma unroll
      for (int i = 0; i < 16; ++i) s += *(LAS float*)(L + F_SSQ + i * 512 + tid * 4);
      *(LAS float*)(L + F_RS + tid * 4) = __builtin_amdgcn_rsqf(s * (1.f / 576.f) + 1e-5f);
    }
    SBAR();
    float rs[4];
    static_for<0, 18>([&](auto ic) AI {
      constexpr int s = decltype(ic)::v;
      constexpr bool nx = s < 17;
      using SP = IC<(s >= 1 ? s + 2 : 99)>;
      using SN = IC<s + 3>;
      grh(s, 0, 0); slot(SP{}, IC<6>{}); rdB(s, 8); SBAR(); grh(s, 0, 1);
      grh(s, 1, 0); slot(SP{}, IC<7>{}); SBAR(); grh(s, 1, 1);
      grh(s, 2, 0); slot(SP{}, IC<8>{}); SBAR(); grh(s, 2, 1);
      if (s == 0) wait_bar<1>(); else if (s == 3) wait_bar<7>(); else if (s == 7) wait_bar<6>(); else if (s <= 15) wait_bar<5>(); else if (s == 16) wait_bar<4>(); else lds_bar();
      if constexpr (s == 2) { warm_gu(IC<F_RS + 1024>{}, 352, 8); SBAR(); }
      if constexpr (s == 6) {
        touch<F_RS + 1024>(rD, (unsigned)(xcd * 576 * 1152 + min(gw * 21 + min(lane, 20), 5183) * 128), lds0 + w * 256);
        SBAR();
      }
      if constexpr (s == 13) { vb0 = mkb(w, true); vb1 = mkb(w + 8, true); vbx = mkb(16 + (w & 1), true); SBAR(); }
      grh(s, 3, 0); slot(SN{}, IC<0>{}); if (nx) { rdA(s + 1, 0); rdA(s + 1, 1); rdB(s + 1, 0); } SBAR(); grh(s, 3, 1);
      grh(s, 4, 0); slot(SN{}, IC<1>{}); if (nx) { rdA(s + 1, 2); rdA(s + 1, 3); rdB(s + 1, 1); } SBAR(); grh(s, 4, 1);
      grh(s, 5, 0); slot(SN{}, IC<2>{}); if (nx) { rdB(s + 1, 2); rdB(s + 1, 3); } SBAR(); grh(s, 5, 1);
      grh(s, 6, 0); slot(SN{}, IC<3>{}); if (nx) { rdB(s + 1, 4); rdB(s + 1, 5); } SBAR(); grh(s, 6, 1);
      grh(s, 7, 0); slot(SN{}, IC<4>{}); if (nx) { rdB(s + 1, 6); }
      if (s == 16) {
#pragma unroll
        for (int m = 0; m < 4; ++m) rs[m] = *(LAS float*)(L + F_RS + (wm * 64 + m * 16 + fr) * 4);
      }
      SBAR(); grh(s, 7, 1);
      grh(s, 8, 0); slot(SN{}, IC<5>{}); SBAR(); grh(s, 8, 1); if (nx) { rdB(s + 1, 7); } SBAR();
    });
    asm volatile("s_nop 7\n\ts_nop 1" ::: "memory");
    SBAR();
    STAMP(20);
    slot(IC<20>{}, IC<6>{}); slot(IC<20>{}, IC<7>{}); slot(IC<20>{}, IC<8>{});
    {
      const bool own = wm == hh;
      const lptr hb = L + F_H;
      const unsigned hx = WS_HX + (mh * 16 + fs) * 36864;
      auto glu = [&](float g, float u, float rsv, float rnv) AI { return __builtin_amdgcn_rcpf(1.f + __builtin_amdgcn_exp2f(g * rnv)) * (u * rsv); };
#pragma unroll
      for (int pr = 0; pr < 2; ++pr) {
#pragma unroll
        for (int m = 0; m < 4; ++m) {
          const float rsv = rs[m], rnv = rsv * -1.4426950408889634f;
          const f32x4 g0 = acc[4 * pr][m], u0 = acc[4 * pr + 1][m], g1 = acc[4 * pr + 2][m], u1 = acc[4 * pr + 3][m];
          u32x4 o;
          o[0] = pk2(glu(g0[0], u0[0], rsv, rnv), glu(g0[1], u0[1], rsv, rnv));
          o[1] = pk2(glu(g0[2], u0[2], rsv, rnv), glu(g0[3], u0[3], rsv, rnv));
          o[2] = pk2(glu(g1[0], u1[0], rsv, rnv), glu(g1[1], u1[1], rsv, rnv));
          o[3] = pk2(glu(g1[2], u1[2], rsv, rnv), glu(g1[3], u1[3], rsv, rnv));
          const int off = (2 * wn + pr) * 4096 + m * 1024 + lane * 16;
          if (own) *(LAS u32x4*)(hb + off) = o; else st16_dev(rW, hx + off, o);
        }
      }
      const bool up = lane >= 32;
#pragma unroll
      for (int mp = 0; mp < 2; ++mp) {
        float g[4], u[4];
#pragma unroll
        for (int rr = 0; rr < 4; ++rr) {
          auto sw = __builtin_amdgcn_permlane32_swap(__float_as_uint(acc[8][2 * mp][rr]), __float_as_uint(acc[8][2 * mp + 1][rr]), false, false);
          g[rr] = __uint_as_float(sw[0]);
          u[rr] = __uint_as_float(sw[1]);
        }
        const float rsv = up ? rs[2 * mp + 1] : rs[2 * mp], rnv = rsv * -1.4426950408889634f;
        const int m = 2 * mp + (up ? 1 : 0);
        const int gam = 2 * wn + gb;
        const int off = 8 * 4096 + m * 1024 + (fr + 16 * (gam & 3)) * 16 + (gam >> 2) * 8;
        const u32x2 o = {pk2(glu(g[0], u[0], rsv, rnv), glu(g[1], u[1], rsv, rnv)), pk2(glu(g[2], u[2], rsv, rnv), glu(g[3], u[3], rsv, rnv))};
        if (own) *(LAS u32x2*)(hb + off) = o; else st8_dev(rW, hx + off, o);
      }
    }
    STAMP(21);
    wait_bar<8>();
    lptr pD[4];
    {
      const int rho = 4 * fq + q4;
      pD[0] = L + rho * 128 + ((wn ^ ((rho >> 1) & 3)) * 32) + 8 * pp;
      pD[1] = pD[0] + f_wslot(1); pD[2] = pD[0] + f_wslot(2); pD[3] = pD[0] + f_wslot(3);
      PIN(pD[1]); PIN(pD[2]); PIN(pD[3]);
    }
    auto rdD = [&](int c, int k) AI { B[k] = cat8(trd(pD[f_reg(c)] + k * 4096), trd(pD[f_reg(c)] + k * 4096 + 2048)); };
    bf16x8 HA[9][2];
    lptr hr = L + F_H + wm * 2048 + lane * 16;
    auto rdH = [&](int k) AI { HA[k][0] = ldfrag(hr + k * 4096); HA[k][1] = ldfrag(hr + k * 4096 + 1024); };
#pragma unroll
    for (int k = 0; k < 9; ++k) { if (k < 8) rdD(0, k); rdH(k); }
    hr -= F_H;
    const unsigned part = WS_PART + (((fs >> 1) * 32 + t64o) * 72 + w) * 1024 + lane * 16;
    const unsigned f4 = WS_F4 + (t64o * 8 + (fs >> 1)) * 64;
    const unsigned vhx = WS_HX + (mh * 16 + (fs ^ 1)) * 36864 + tid * 16;
    f32x4 o[9][2];
    auto st1 = [&](int cb) AI {
      const f32x4 a = o[cb][0], b = o[cb][1];
      __builtin_amdgcn_raw_buffer_store_b128((u32x4){pk2(a[0], a[1]), pk2(a[2], a[3]), pk2(b[0], b[1]), pk2(b[2], b[3])}, rW, part, cb * 8192, 16);
    };
    auto gd = [&](int c, int k) AI {
#pragma unroll
      for (int m = 0; m < 2; ++m) o[c % 9][m] = MFMA16(B[k], HA[k][m], (c < 9 && k == 0) ? z4 : o[c % 9][m]);
      SBAR();
    };
    SBAR();
    const unsigned f5p = WS_F5 + (mh * 16 + (fs ^ 1)) * 64;
    u32x2 pf;
    auto pcs = [&](auto stc, auto nc) AI {
      constexpr int st = decltype(stc)::v;
      static_for<0, decltype(nc)::v>([&](auto ic) AI {
        constexpr int i = decltype(ic)::v;
        if constexpr (st < 18) p2(stc, ic); else dma_dev<i * 8192, i * 8192>(rW, vhx, wl1);
      });
    };
    auto slot2 = [&](auto ivc, auto slc) AI {
      constexpr int iv = decltype(ivc)::v, sl = decltype(slc)::v;
      constexpr int s1 = iv == 0 ? 3 : iv <= 3 ? iv + 4 : iv == 4 ? 18 : iv <= 9 ? iv + 3 : iv + 4;
      if constexpr (iv >= 0 && iv <= 13) {
        if constexpr (sl == LOSL) { if (lo) { pcs(IC<s1>{}, IC<5>{}); if constexpr (iv == 0 || iv == 9) pcs(IC<s1 + 1>{}, IC<5>{}); } }
        if constexpr (sl == HISL) { if (!lo) { pcs(IC<s1>{}, IC<4>{}); if constexpr (iv == 0 || iv == 9) pcs(IC<s1 + 1>{}, IC<4>{}); } }
      }
    };
    static_for<0, 18>([&](auto ic) AI {
      constexpr int c = decltype(ic)::v;
      constexpr bool nx = c < 17;
      using P = IC<c - 1>;
      gd(c, 0); slot2(P{}, IC<6>{}); rdD(c, 8); if constexpr (c == 8) rdH(0); if constexpr (c == 9) rdH(8); SBAR();
      gd(c, 1); slot2(P{}, IC<7>{}); if constexpr (c == 8) rdH(1); SBAR();
      gd(c, 2); slot2(P{}, IC<8>{}); if constexpr (c == 8) rdH(2); if constexpr (c >= 10) st1(c - 10); SBAR();
      constexpr int N = c == 0 ? 4 : c == 3 ? 9 : c <= 6 ? 8 : c <= 9 ? 4 : c == 10 ? 9 : c <= 14 ? 10 : c == 15 ? 6 : 2;
      if constexpr (c == 4) asm volatile("s_waitcnt vmcnt(8) lgkmcnt(0)\n\ts_barrier" : "+v"(pf)::"memory");
      else wait_bar<N>();
      if constexpr (c == 0) STAMP(26);
      if constexpr (c == 3) STAMP(27);
      if constexpr (c == 4) STAMP(28);
      if constexpr (c == 8) STAMP(29);
      if constexpr (c == 12) STAMP(30);
      if constexpr (c == 16) STAMP(31);
      if constexpr (c == 1) { if (tid == 0) st8_dev(rW, WS_F5 + (mh * 16 + fs) * 64, (u32x2){tag0, tag1}); }
      if constexpr (c >= 12) { if (tid == 0) st8_dev(rW, f4, (u32x2){tag0 + (c - 11), tag1}); }
      gd(c, 3);
      if constexpr (c == 2) asm volatile("buffer_load_dwordx2 %0, %1, %2, 0 offen sc1" : "=v"(pf) : "v"(f5p), "s"(rW) : "memory");
      if constexpr (c == 4) { if (!__all(pf[0] == tag0 && pf[1] == tag1)) poll(rW, f5p, tag0, tag1, 0); }
      slot2(ic, IC<0>{}); if (nx) { rdD(c + 1, 0); rdD(c + 1, 1); } if constexpr (c == 8) rdH(3); SBAR();
      gd(c, 4); slot2(ic, IC<1>{}); if (nx) { rdD(c + 1, 2); rdD(c + 1, 3); } if constexpr (c == 8) rdH(4); SBAR();
      gd(c, 5); slot2(ic, IC<2>{}); if (nx) rdD(c + 1, 4); if constexpr (c == 8) rdH(5); SBAR();
      gd(c, 6); slot2(ic, IC<3>{}); if (nx) rdD(c + 1, 5); if constexpr (c == 8) rdH(6); SBAR();
      gd(c, 7); slot2(ic, IC<4>{}); if (nx) rdD(c + 1, 6); if constexpr (c == 8) rdH(7); SBAR();
      gd(c, 8); slot2(ic, IC<5>{}); if (nx) rdD(c + 1, 7); SBAR();
    });
    st1(8);
    STAMP(22);
    asm volatile("s_waitcnt vmcnt(0)\n\ts_barrier" ::: "memory");
    if (tid == 0) st8_dev(rW, f4, (u32x2){tag0 + 9, tag1});
    STAMP(23);
  }

  {
    const int q = fs >> 1, wm = q & 1, wn = q >> 1, ms0 = hh * 4 + wm * 2;
    u32x2 hc[2];
    float mu[2];
    auto resid = [&](int c) AI {
      const int cg = 4 * (c * 4 + wn) + fq, j = cg / 18, gl = cg - 18 * j;
      const unsigned ho = WS_HCF + ((mh * 18 + (gl < 16 ? 2 * j + (gl >> 3) : 16 + (j >> 2))) * 8 + ms0) * 1024 +
                          (fr + 16 * (gl < 16 ? (gl & 3) : (j & 3))) * 16 + (gl < 16 ? ((gl >> 2) & 1) : gl - 16) * 8;
      hc[0] = ld8_dev(rW, ho);
      hc[1] = ld8_dev(rW, ho + 1024);
    };
    auto fin = [&](int c, const float(&s)[8]) AI {
#pragma unroll
      for (int i = 0; i < 2; ++i) {
        const u32x2 o = {pk2(s[4 * i] + (bflo(hc[i][0]) + mu[i]), s[4 * i + 1] + (bfhi(hc[i][0]) + mu[i])),
                         pk2(s[4 * i + 2] + (bflo(hc[i][1]) + mu[i]), s[4 * i + 3] + (bfhi(hc[i][1]) + mu[i]))};
        *(u32x2*)(out + (size_t)(mh * 128 + (ms0 + i) * 16 + fr) * 576 + (c * 4 + wn) * 16 + 4 * fq) = o;
      }
    };
    poll(rW, WS_F4 + (t64o * 8 + (lane & 7)) * 64, tag0, tag1, w + 1);
    const unsigned vo = WS_PART + (t64o * 72 + q) * 1024 + lane * 16;
    u32x4 v[8];
#pragma unroll
    for (int f = 0; f < 8; ++f) v[f] = ld16_dev(rW, vo + w * 8192, f * 2304 * 1024);
    resid(w);
#pragma unroll
    for (int i = 0; i < 2; ++i) mu[i] = __uint_as_float(ld4_dev(rW, WS_MEAN2 + (mh * 128 + (ms0 + i) * 16 + fr) * 4));
    float s[8];
#pragma unroll
    for (int k = 0; k < 8; ++k) s[k] = 0.f;
#pragma unroll
    for (int f = 0; f < 8; ++f)
#pragma unroll
      for (int k = 0; k < 4; ++k) { s[2 * k] += bflo(v[f][k]); s[2 * k + 1] += bfhi(v[f][k]); }
    fin(w, s);
    STAMP(24);
    poll(rW, WS_F4 + (t64o * 8 + w) * 64, tag0, tag1, 9);
    const u32x4 a = ld16_dev(rW, vo + 8 * 8192 + w * 2304 * 1024, 0);
    if (w == 0) resid(8);
    *(LAS u32x4*)(L + w * 1024 + lane * 16) = a;
    lds_bar();
    if (w == 0) {
#pragma unroll
      for (int k = 0; k < 8; ++k) s[k] = 0.f;
#pragma unroll
      for (int f = 0; f < 8; ++f) {
        const u32x4 t = *(LAS u32x4*)(L + f * 1024 + lane * 16);
#pragma unroll
        for (int k = 0; k < 4; ++k) { s[2 * k] += bflo(t[k]); s[2 * k + 1] += bfhi(t[k]); }
      }
      fin(8, s);
    }
    STAMP(25);
  }
}

extern "C" void kernel_launch(void* const* d_in, const int* in_sizes, int n_in, void* d_out, int out_size, void* d_ws, size_t ws_size,
                              hipStream_t stream) {
  mk_block<<<256, 512, 0, stream>>>((const u16*)d_in[0], (const u16*)d_in[1], (const u16*)d_in[2], (const u16*)d_in[4],
                                    (const u16*)d_in[5], (const u16*)d_in[6], (char*)d_ws, (u16*)d_out);
}
```

```cpp
#include <hip/hip_runtime.h>
#include <hip/hip_bf16.h>
#include <stdint.h>

#define PUBSTEP 8
#define F2V 0
#define LOSL 0
#define HISL 8
#define CODE_BYTES 70656
#define B2POS 2
#define WOE 5
#define WSCH 1
#define PRIO 2
#define DOTST 0
#define ABL 0
typedef unsigned short u16;
typedef short bf16x8 __attribute__((ext_vector_type(8)));
typedef short s16x4 __attribute__((ext_vector_type(4)));
typedef float f32x4 __attribute__((ext_vector_type(4)));
typedef float f32x2 __attribute__((ext_vector_type(2)));
typedef unsigned u32x4 __attribute__((ext_vector_type(4)));
typedef unsigned u32x2 __attribute__((ext_vector_type(2)));
typedef __bf16 bf16x2_t __attribute__((ext_vector_type(2)));
typedef __amdgpu_buffer_rsrc_t rsrc_t;
#define LAS __attribute__((address_space(3)))
#define DI __device__ __forceinline__
#define AI __attribute__((always_inline))
typedef LAS char* lptr;

#define MFMA16(a, b, c) __builtin_amdgcn_mfma_f32_16x16x32_bf16((a), (b), (c), 0, 0, 0)
#define SBAR() __builtin_amdgcn_sched_barrier(0)
#define PIN(x) asm volatile("" : "+v"(x))
#define MFMA_ACC(c, a, b) asm("v_mfma_f32_16x16x32_bf16 %0, %1, %2, %0" : "+v"(c) : "v"(a), "v"(b))
#define MFMA_NEW(c, a, b) asm("v_mfma_f32_16x16x32_bf16 %0, %1, %2, 0" : "=&v"(c) : "v"(a), "v"(b))

constexpr int REC = 3328;
constexpr int WS_EPOCH = 0;
constexpr int WS_F1 = 256;
constexpr int WS_F2 = WS_F1 + 256 * 64;
constexpr int WS_F3 = WS_F2 + 256 * 64;
constexpr int WS_F4 = WS_F3 + 256 * 64;
constexpr int WS_F5 = WS_F4 + 256 * 64;
constexpr int WS_REC = WS_F5 + 256 * 64;
constexpr int WS_PM = WS_REC + 256 * REC;
constexpr int WS_AF = WS_PM + 16 * 8192;
constexpr int WS_HCF = WS_AF + 2048 * 576 * 2;
constexpr int WS_SSQ = WS_HCF + 2048 * 576 * 2;
constexpr int WS_MEAN2 = WS_SSQ + 16 * 8192;
constexpr int WS_HX = WS_MEAN2 + 8192;
constexpr int WS_PART = WS_HX + 256 * 36864;
constexpr int WS_END = WS_PART + 8 * 2048 * 576 * 2;

template <int I> struct IC { static constexpr int v = I; };
template <int I, int N, class Fn> DI void static_for(Fn&& f) {
  if constexpr (I < N) { f(IC<I>{}); static_for<I + 1, N>(f); }
}
DI unsigned pk2(float a, float b) {
  bf16x2_t v = {(__bf16)a, (__bf16)b};
  return __builtin_bit_cast(unsigned, v);
}
DI float bflo(unsigned w) { return __uint_as_float(w << 16); }
DI float bfhi(unsigned w) { return __uint_as_float(w & 0xffff0000u); }
DI s16x4 trd(lptr p) { return __builtin_bit_cast(s16x4, __builtin_amdgcn_ds_read_tr16_b64_v4i16((LAS s16x4*)p)); }
DI bf16x8 cat8(s16x4 lo, s16x4 hi) { return __builtin_shufflevector(lo, hi, 0, 1, 2, 3, 4, 5, 6, 7); }
DI bf16x8 ldfrag(lptr p) { return *(LAS bf16x8*)p; }
DI rsrc_t mkrs(const void* p, int bytes) { return __builtin_amdgcn_make_buffer_rsrc((void*)p, (short)0, bytes, 0x00020000); }
template <int LOFF, int GOFF> DI void dma(rsrc_t rs, unsigned voff, unsigned wl) {
  unsigned t;
  asm volatile("s_add_u32 m0, %3, %4\n\ts_mov_b32 %0, %5\n\tbuffer_load_dwordx4 %1, %2, %0 offen lds"
               : "=&s"(t) : "v"(voff), "s"(rs), "s"(wl), "i"(LOFF), "i"(GOFF) : "memory", "scc");
}
template <int LOFF, int GOFF> DI void dma_dev(rsrc_t rs, unsigned voff, unsigned wl) {
  unsigned t;
  asm volatile("s_add_u32 m0, %3, %4\n\ts_mov_b32 %0, %5\n\tbuffer_load_dwordx4 %1, %2, %0 offen sc1 lds"
               : "=&s"(t) : "v"(voff), "s"(rs), "s"(wl), "i"(LOFF), "i"(GOFF) : "memory", "scc");
}
DI void dma_dyn(rsrc_t rs, unsigned voff, unsigned soff, unsigned ldst) {
  asm volatile("s_mov_b32 m0, %3\n\ts_nop 0\n\tbuffer_load_dwordx4 %0, %1, %2 offen sc1 lds" ::"v"(voff), "s"(rs), "s"(soff), "s"(ldst) : "memory");
}
template <int LOFF> DI void touch(rsrc_t rs, unsigned voff, unsigned wl) {
  asm volatile("s_add_u32 m0, %2, %3\n\ts_nop 0\n\tbuffer_load_dword %0, %1, 0 offen lds" ::"v"(voff), "s"(rs), "s"(wl), "i"(LOFF) : "memory", "scc");
}
DI void st16_dev(rsrc_t rs, unsigned voff, u32x4 v) { __builtin_amdgcn_raw_buffer_store_b128(v, rs, voff, 0, 16); }
DI void st8_dev(rsrc_t rs, unsigned voff, u32x2 v) { __builtin_amdgcn_raw_buffer_store_b64(v, rs, voff, 0, 16); }
DI void st4_dev(rsrc_t rs, unsigned voff, unsigned v) { __builtin_amdgcn_raw_buffer_store_b32(v, rs, voff, 0, 16); }
DI u32x4 ld16_dev(rsrc_t rs, unsigned voff, int soff) { return __builtin_amdgcn_raw_buffer_load_b128(rs, voff, soff, 16); }
DI u32x2 ld8_dev(rsrc_t rs, unsigned voff) { return __builtin_amdgcn_raw_buffer_load_b64(rs, voff, 0, 16); }
DI unsigned ld4_dev(rsrc_t rs, unsigned voff) { return __builtin_amdgcn_raw_buffer_load_b32(rs, voff, 0, 16); }
template <int N> DI void wait_bar() { asm volatile("s_waitcnt vmcnt(%0) lgkmcnt(0)\n\ts_barrier" ::"i"(N) : "memory"); }
DI void lds_bar() { asm volatile("s_waitcnt lgkmcnt(0)\n\ts_barrier" ::: "memory"); }
DI float phi_f(float t) { return (t > 0.f ? t + 1.f : __builtin_amdgcn_exp2f(t * 1.4426950408889634f)) + 1e-6f; }
DI float xor16(float v) { auto r = __builtin_amdgcn_permlane16_swap(__float_as_uint(v), __float_as_uint(v), false, false); return __uint_as_float(r[0]) + __uint_as_float(r[1]); }
DI float xor32(float v) { auto r = __builtin_amdgcn_permlane32_swap(__float_as_uint(v), __float_as_uint(v), false, false); return __uint_as_float(r[0]) + __uint_as_float(r[1]); }
DI float sum_fq(float v) { return xor32(xor16(v)); }
DI void poll(rsrc_t rs, unsigned voff, unsigned t0, unsigned t1, unsigned need) {
#pragma unroll 1
  for (unsigned spin = 0; spin < (1u << 17); ++spin) {
    u32x2 f;
    asm volatile("buffer_load_dwordx2 %0, %1, %2, 0 offen sc1\n\ts_waitcnt vmcnt(0)" : "=v"(f) : "v"(voff), "s"(rs) : "memory");
    const unsigned d = f[0] - t0;
    if (__all(f[1] == t1 && d >= need && d < 16u)) break;
    __builtin_amdgcn_s_sleep(1);
  }
}

#define STAMP_DECL
#define STAMP(i) do {} while (0)

constexpr int KA_SLOT = 16384 + 10240;
constexpr int KA_KVO = 16384;
constexpr int ka_q(int kb) { return kb < 5 ? 4 * KA_SLOT + kb * 5120 : (kb - 5) * 5120; }
constexpr int KA_EX = KA_SLOT;
constexpr int KB_WO = 20480;
constexpr int KA_WO = 5 * KA_SLOT;
constexpr int KA_CS = KA_WO + 3072;
constexpr int KA_KVI = KA_CS + 8 * 320;
constexpr int KA_RSP = KA_KVI + 6144;
constexpr int KA_ZZ = KA_RSP + 160;
constexpr int KA_RS = KA_ZZ + 160;
constexpr int KA_QC = KA_RS + 320;
constexpr int KA_DEAD = KA_QC + 768;
static_assert(KA_DEAD + 2048 <= 163840, "lds");

struct Tab { unsigned v[1024]; };
constexpr Tab make_tab() {
  Tab T{};
  for (int t = 0; t < 512; ++t) {
    const int w = t >> 6, ln = t & 63, fr = ln & 15, fq = ln >> 4;
    const int slot = w == 0 ? ln : w == 3 ? 64 + ln : w == 1 ? (fr < 2 ? 128 + fq * 2 + fr : -1) : w == 2 ? (fq == 0 ? 136 + fr : -1)
                   : w == 4 ? (fr == 8 ? 152 + fq : (ln < 8 ? 197 + ln : ln == 9 ? 205 : -1)) : w == 5 ? ((fr >= 4 && fr <= 8) ? 156 + fq * 5 + fr - 4 : -1)
                   : w == 6 ? (fq == 0 ? 176 + fr : -1) : ((fq == 0 && fr >= 4 && fr <= 8) ? 192 + fr - 4 : -1);
    int ce = 0, cd = 0;
    if (t < 128) { ce = (t & 15) + (t >> 6) * 16; cd = ((t >> 4) & 3) * 4 + (t >> 6) * 16; }
    else if (t < 136) { ce = 16 + (t & 1); cd = ((t - 128) >> 1) * 4; }
    else if (t < 152) { ce = t - 136; cd = 16; }
    else if (t < 156) { ce = 36; cd = (t - 152) * 4; }
    else if (t < 176) { const int i = t - 156, f = i / 5; ce = 32 + i - f * 5; cd = 16 + 4 * f; }
    else if (t < 192) { ce = t - 160; cd = 32; }
    else if (t < 197) { ce = 32 + t - 192; cd = 32; }
    int m = 0;
    for (int rr = 0; rr < 4; ++rr) m |= ((cd + rr >= 18) == (ce >= 18)) ? (1 << rr) : 0;
    int dst = KA_KVI + ce * 96 + cd * 2;
    if (ce == 36) m = 16 | (~m & 15);
    if (t >= 197) { dst = KA_RS + (t < 206 ? (t - 197) * 16 : 0); m = t < 206 ? 32 : 64; }
    T.v[2 * t] = slot >= 0 ? (unsigned)slot * 16u : 0x40000000u;
    T.v[2 * t + 1] = (unsigned)dst | ((unsigned)m << 24);
  }
  return T;
}
static __device__ const Tab g_tab = make_tab();

constexpr int F_G = 8192, F_U = 8192 + 18432 + 16, F_STG = F_U + 18432;
constexpr int F_SSQ = 3 * F_STG;
constexpr int F_RS = F_SSQ + 8192;
constexpr int F_H = 163840 - 73728;
constexpr int f_wslot(int i) { return i == 0 ? 0 : i == 1 ? F_STG : i == 2 ? F_H + 36864 : F_H; }
constexpr int f_reg(int c) { return c < 8 ? (c & 3) : c == 8 ? 1 : c == 9 ? 2 : c == 10 ? 3 : c == 11 ? 1 : c == 12 ? 2 : c == 13 ? 0 : c == 14 ? 3 : c == 15 ? 1 : c == 16 ? 2 : 0; }

__global__ __launch_bounds__(512, 2) void mk_block(const u16* __restrict__ x, const u16* __restrict__ wqkv, const u16* __restrict__ wo,
                                                   const u16* __restrict__ wg, const u16* __restrict__ wu, const u16* __restrict__ wd,
                                                   char* __restrict__ ws, u16* __restrict__ out) {
  __shared__ __attribute__((aligned(16))) char smem[163840];
  STAMP_DECL; STAMP(0);
  const lptr L = (lptr)smem;
  const unsigned lds0 = (unsigned)(uintptr_t)L;
  const int tid = threadIdx.x, lane = tid & 63, w = __builtin_amdgcn_readfirstlane(tid >> 6);
  const int fr = lane & 15, fq = lane >> 4, q4 = fr >> 2, pp = lane & 3;
  const int xcd = blockIdx.x & 7, r = blockIdx.x >> 3;
  const unsigned wl1 = lds0 + w * 1024, wl2 = lds0 + w * 2048;
  const rsrc_t rW = mkrs(ws, WS_END), rX = mkrs(x, 2048 * 576 * 2);
  const rsrc_t rG = mkrs(wg, 576 * 4608 * 2), rU = mkrs(wu, 576 * 4608 * 2);
  const f32x4 z4 = {0.f, 0.f, 0.f, 0.f};
  unsigned tag0, tag1;
  const int gw = r * 8 + w;
  auto warm_gu = [&](auto lc, int row0, int nl) AI {
    const int li = gw * nl + min(lane, nl - 1), row = li / 9;
    const unsigned off = (unsigned)((row0 + row) * 9216 + (li - row * 9) * 128 + xcd * 1152);
    touch<decltype(lc)::v>(rG, off, lds0 + w * 256);
    touch<decltype(lc)::v>(rU, off, lds0 + w * 256);
  };

  const rsrc_t rO = mkrs(wo, 576 * 576 * 2);
  auto mko = [&](int pi, bool left) AI {
    const int c = pi * 64 + lane, row = c / 9, ch = c - row * 9, st = row >> 5, rho = row & 31, rr = rho & 15;
    const int t = rho >> 4, g = 2 * (rr & 1) + (rr >> 3), q = (rr >> 1) & 3;
    const int k = left ? 36 * (8 * st + 4 * t + g) + 32 + q : 36 * st + 16 * t + 4 * g + q;
    return (unsigned)(k * 1152 + ch * 16 + 144 * ((xcd >> 2) * 4 + (r >> 3)));
  };
  {
    const rsrc_t rQ = mkrs(wqkv, 576 * 1728 * 2);
    const int tt = (xcd & 3) * 4 + (r & 3), p = (xcd >> 2) * 8 + (r >> 2);
    const int o8 = (p & 1) * 8, ot = (tt & 1) * 8;
    const int tok0 = tt * 128 + w * 16;
    const unsigned vx = (unsigned)((tok0 + (lane >> 3)) * 1152 + (((lane & 7) ^ (lane >> 3)) * 16));
    {
      const unsigned g = blockIdx.x * 8 + w;
      touch<KA_DEAD>(mkrs((const void*)(__builtin_amdgcn_s_getpc() & ~127ull), CODE_BYTES), (unsigned)(gw * 3 + min(lane, 2)) * 128, lds0 + w * 256);
      touch<KA_DEAD>(rX, lane < 9 ? (g * 9 + lane) * 128 : 0x7fffff00u, lds0 + w * 256);
      touch<KA_DEAD>(rQ, lane < 8 ? (g * 8 + lane) * 128 : 0x7fffff00u, lds0 + w * 256);
      touch<KA_DEAD>(rO, lane < 3 ? (g * 3 + lane) * 128 : 0x7fffff00u, lds0 + w * 256);
    }
    dma<0, 0>(rX, vx, wl2);
    dma<1024, 8 * 1152>(rX, vx, wl2);
    auto mkv = [&](int pi) AI {
      const int c = pi * 64 + lane, row = c / 10, cc = c - row * 10, rho = row & 31;
      const int k = (row & 32) + ((rho >> 2) & 3) * 8 + (rho >> 4) * 4 + (rho & 3);
      return (unsigned)(k * 3456 + (cc >= 5 ? 1152 - 80 : 0) + cc * 16 + 1152 + 72 * p - o8);
    };
    const unsigned vkv0 = mkv(w), vkv8 = mkv(8 + (w & 1));
    const bool xtra = w >= 6;
    const unsigned wlx = lds0 + (w & 1) * 1024;
    auto piece = [&](auto kbc, int i) AI {
      constexpr int kb = decltype(kbc)::v, so = (kb % 5) * KA_SLOT;
      if ((ABL == 2 || ABL == 9) && kb >= 3) return;
      if (i == 0) dma<so, kb * 128>(rX, vx, wl2);
      if (i == 1) dma<so + 1024, kb * 128 + 8 * 1152>(rX, vx, wl2);
      if (i == 2) dma<so + KA_KVO, kb * 64 * 3456>(rQ, vkv0, wl1);
      if (i == 3) { if (xtra) dma<so + KA_KVO + 8192, kb * 64 * 3456>(rQ, vkv8, wlx); }
    };
    piece(IC<0>{}, 2); piece(IC<0>{}, 3);
    piece(IC<1>{}, 0); piece(IC<1>{}, 1); piece(IC<1>{}, 2); piece(IC<1>{}, 3);
    u32x2 ep, tab;
    asm volatile("buffer_load_dwordx2 %0, off, %1, 0 sc1" : "=v"(ep) : "s"(rW) : "memory");
    asm volatile("global_load_dwordx2 %0, %1, %2" : "=v"(tab) : "v"(tid * 8), "s"(g_tab.v) : "memory");
    if (w < 3) {
      const int c = min(tid, 179), rw = c / 5;
      dma<KA_WO, 0>(rO, (unsigned)((36 * p + rw) * 1152 + 72 * tt - ot + (c - rw * 5) * 16), wl1);
    }
    piece(IC<2>{}, 0); piece(IC<2>{}, 1); piece(IC<2>{}, 2); piece(IC<2>{}, 3);
    if (tid < 384) *(LAS u32x4*)(L + KA_KVI + tid * 16) = (u32x4){0u, 0u, 0u, 0u};
    unsigned vq;
    {
      const int c = (w < 5 ? w : 0) * 64 + lane, row = c / 5, ch = c - row * 5;
      const int rho = row & 31, rr = rho & 15;
      vq = (unsigned)(((row & 32) + 8 * (2 * (rr & 1) + (rr >> 3)) + 4 * (rho >> 4) + ((rr >> 1) & 3)) * 3456 + ch * 16 + 72 * p - o8);
    }
    lptr bk[3], bx[3], ba0[3], ba1[3];
    bk[0] = L + KA_KVO + (4 * fq + q4) * 160 + o8 + 8 * pp;
    bx[0] = L + KA_KVO + (4 * fq + q4) * 160 + 80 * (pp & 1) + o8 + 64;
    ba0[0] = L + w * 2048 + fr * 128 + (fq ^ (fr & 7)) * 16;
    ba1[0] = L + w * 2048 + fr * 128 + ((4 + fq) ^ (fr & 7)) * 16;
#pragma unroll
    for (int i = 1; i < 3; ++i) {
      bk[i] = bk[0] + 2 * i * KA_SLOT; bx[i] = bx[0] + 2 * i * KA_SLOT; ba0[i] = ba0[0] + 2 * i * KA_SLOT; ba1[i] = ba1[0] + 2 * i * KA_SLOT;
      PIN(bk[i]); PIN(bx[i]); PIN(ba0[i]); PIN(ba1[i]);
    }
    f32x4 acc[5], cs[5], gram = z4, sx = z4;
#pragma unroll
    for (int i = 0; i < 5; ++i) acc[i] = cs[i] = z4;
    bf16x8 ones = {0x3F80, 0x3F80, 0x3F80, 0x3F80, 0x3F80, 0x3F80, 0x3F80, 0x3F80};
    PIN(ones);
    bf16x8 a[18], B[2][5];
    auto readA = [&](int kb) AI {
      const int sl = kb % 5;
      a[2 * kb] = ldfrag(ba0[sl >> 1] + (sl & 1) * KA_SLOT);
      a[2 * kb + 1] = ldfrag(ba1[sl >> 1] + (sl & 1) * KA_SLOT);
    };
    auto readB = [&](int s, int i) AI {
      const int sl = (s >> 1) % 5, so = (sl & 1) * KA_SLOT + (s & 1) * 5120;
      if ((ABL == 3 || ABL == 9) && s >= 2) return;
      if (i < 4) {
        const int off = so + (i >> 1) * 80 + 32 * (i & 1);
        B[s & 1][i] = cat8(trd(bk[sl >> 1] + off), trd(bk[sl >> 1] + off + 2560));
      } else B[s & 1][4] = cat8(trd(bx[sl >> 1] + so), trd(bx[sl >> 1] + so + 2560));
    };
    auto issue_q = [&](auto kc) AI {
      constexpr int k = decltype(kc)::v;
      dma<ka_q(k), k * 64 * 3456>(rQ, vq, wl1);
    };
    auto blk = [&](auto bc, int i) AI { piece(bc, i); if (i == 2) piece(bc, 3); };
    auto warm = [&](int h, bool up) AI {
      const int li = min((h * 256 + gw) * 4 + min(lane, 3), 1943), row = li / 9;
      touch<KA_DEAD>(up ? rU : rG, (unsigned)(row * 9216 + (li - row * 9) * 128 + xcd * 1152), lds0 + w * 256);
    };
    auto slot = [&](auto gc) AI {
      constexpr int g = decltype(gc)::v;
      if constexpr (g < 3) { blk(IC<3>{}, g); }
      if constexpr (g >= 3 && g < 6) blk(IC<4>{}, g - 3);
      if constexpr (g >= 6 && g < 9) blk(IC<5>{}, g - 6);
      if constexpr (g >= 15 && g < 18) blk(IC<6>{}, g - 15);
      if constexpr (g >= 18 && g < 21) blk(IC<7>{}, g - 18);
      if constexpr (g == 22) warm(0, false);
      if constexpr (g == 25) warm(0, true);
      if constexpr (g >= 27 && g < 30) blk(IC<8>{}, g - 27);
      if constexpr (g >= 30 && g < 35) { if (w < 5) issue_q(IC<g - 30>{}); }
      if constexpr (g == 35) warm(1, false);
      if constexpr (g == 38) warm(1, true);
      if constexpr (g >= 39 && g < 43) { if (w < 5) issue_q(IC<g - 34>{}); }
    };
    auto step = [&](auto sc) AI {
      constexpr int s = decltype(sc)::v;
      constexpr bool nx = s < 17;
      acc[0] = MFMA16(B[s & 1][0], a[s], acc[0]); acc[1] = MFMA16(B[s & 1][1], a[s], acc[1]); SBAR();
      if constexpr (nx && (s & 1)) readA((s + 1) >> 1);
      if constexpr (nx) { readB(s + 1, 0); readB(s + 1, 1); }
      slot(IC<3 * s>{}); SBAR();
      acc[2] = MFMA16(B[s & 1][2], a[s], acc[2]); acc[3] = MFMA16(B[s & 1][3], a[s], acc[3]); SBAR();
      if constexpr (nx) { readB(s + 1, 2); readB(s + 1, 3); }
      slot(IC<3 * s + 1>{}); SBAR();
      acc[4] = MFMA16(B[s & 1][4], a[s], acc[4]);
      gram = MFMA16(a[s], a[s], gram); sx = MFMA16(ones, a[s], sx);
      SBAR();
      if constexpr (nx) readB(s + 1, 4);
      slot(IC<3 * s + 2>{}); SBAR();
      if (s >= 13 || (w & 1) == (s & 1)) {
#pragma unroll
        for (int i = 0; i < 5; ++i) cs[i] = MFMA16(B[s & 1][i], ones, cs[i]);
      }
      SBAR();
    };
    STAMP(1);
    wait_bar<8>();
    STAMP(2);
    readA(0);
#pragma unroll
    for (int i = 0; i < 5; ++i) readB(0, i);
    f32x4 csp[5];
    static_for<0, 9>([&](auto ic) AI {
      constexpr int KB = decltype(ic)::v;
      step(IC<2 * KB>{});
      if constexpr (KB == 2) {
        if (tid < 36) {
          float s = 0.f;
#pragma unroll
          for (int i = 0; i < 9; ++i) {
            const u32x2 v = *(LAS u32x2*)(L + KA_WO + tid * 80 + ot + i * 8);
            s += (bflo(v[0]) + bfhi(v[0])) + (bflo(v[1]) + bfhi(v[1]));
          }
          *(LAS float*)(L + KA_RSP + tid * 4) = s;
        }
      }
      if constexpr (KB == 6) {
        if (fr == 0) {
#pragma unroll
          for (int i = 0; i < 5; ++i) *(LAS f32x4*)(L + KA_CS + w * 320 + (i * 4 + fq) * 16) = cs[i];
        }
      }
      if constexpr (KB == 0) wait_bar<3>();
      if constexpr (KB == 2) wait_bar<3>();
      if constexpr (KB == 4) wait_bar<5>();
      if constexpr (KB == 6) { if (w < 5) wait_bar<7>(); else wait_bar<2>(); }
      if constexpr (KB == 6) {
#pragma unroll
        for (int i = 0; i < 5; ++i) csp[i] = *(LAS f32x4*)(L + KA_CS + (w ^ 1) * 320 + (i * 4 + fq) * 16);
      }
      step(IC<2 * KB + 1>{});
    });
    STAMP(3);
    float meanT, rstdT;
    {
      const int rr = fr & 3;
      float g = rr == 0 ? gram[0] : rr == 1 ? gram[1] : rr == 2 ? gram[2] : gram[3];
      g = sum_fq(q4 == fq ? g : 0.f);
      meanT = sx[0] * (1.f / 576.f);
      rstdT = __builtin_amdgcn_rsqf(fmaxf(g * (1.f / 576.f) - meanT * meanT, 0.f) + 1e-5f);
      const lptr exw = L + KA_EX + (w * 16 + fr) * 160 + fq * 8;
#pragma unroll
      for (int i = 0; i < 5; ++i) {
        float v[4];
#pragma unroll
        for (int k = 0; k < 4; ++k) {
          const float t = (acc[i][k] - meanT * (cs[i][k] + csp[i][k])) * rstdT;
          if (i < 2) v[k] = phi_f(t);
          else if (i < 4) v[k] = t;
          else { const float ph = phi_f(t); v[k] = fq == 0 ? ph : (fq == 1 ? t : 1.0f); }
        }
        *(LAS u32x2*)(exw + i * 32) = (u32x2){pk2(v[0], v[1]), pk2(v[2], v[3])};
      }
    }
    STAMP(4);
    asm volatile("s_waitcnt vmcnt(0) lgkmcnt(0)\n\ts_barrier" : "+v"(ep), "+v"(tab)::"memory");
    const unsigned recb = WS_REC + p * 16 * REC;
    {
      const int sa = (0x44101100 >> (4 * w)) & 7, sb = (0x43443232 >> (4 * w)) & 7;
      const lptr ex = L + KA_EX + (4 * fq + q4) * 160 + pp * 8;
      const lptr exa = ex + sa * 32, exb = ex + sb * 32;
      bf16x8 fa[4], fb[4];
#pragma unroll
      for (int k = 0; k < 4; ++k) {
        fa[k] = cat8(trd(exa + k * 5120), trd(exa + k * 5120 + 2560));
        fb[k] = cat8(trd(exb + k * 5120), trd(exb + k * 5120 + 2560));
      }
      const f32x4 rsp = *(LAS f32x4*)(L + KA_RSP + min(lane, 8) * 16);
      f32x4 T = z4;
#pragma unroll
      for (int k = 0; k < 4; ++k) T = MFMA16(fa[k], fb[k], T);
      if (tab[0] >= 197 * 16) T = rsp;
      st16_dev(rW, recb + tt * REC + tab[0], __builtin_bit_cast(u32x4, T));
    }
    STAMP(5);
    tag0 = __builtin_amdgcn_readfirstlane((ep[0] * 0x9E3779B1u + 0x7F4A7C15u + ep[1]) & ~15u);
    tag1 = __builtin_amdgcn_readfirstlane((ep[1] ^ (ep[0] >> 7)) * 0x85EBCA6Bu + 0x165667B1u);
    if (tag1 == (unsigned)__builtin_amdgcn_readfirstlane(ep[1])) tag1 ^= 0x5bd1e995u;
    f32x4 qa[3];
    {
#pragma unroll
      for (int i = 0; i < 3; ++i) qa[i] = z4;
      lptr bq[2], bqp[2];
      bq[0] = L + (2 * (4 * (fq & 1) + q4) + (fq >> 1)) * 80 + o8 + 64;
      bqp[0] = bq[0] - 64 + 8 * pp;
      bq[1] = bq[0] + 4 * KA_SLOT; bqp[1] = bqp[0] + 4 * KA_SLOT;
      PIN(bq[1]); PIN(bqp[1]);
      bf16x8 b[3][3];
      auto rq = [&](int s) AI {
        const int h = s < 10 ? 1 : 0, o = (s < 10 ? s : s - 10) * 2560;
        b[s % 3][0] = cat8(trd(bqp[h] + o), trd(bqp[h] + o + 1280));
        b[s % 3][1] = cat8(trd(bqp[h] + o + 32), trd(bqp[h] + o + 1280 + 32));
        b[s % 3][2] = cat8(trd(bq[h] + o), trd(bq[h] + o + 1280));
      };
      if (w >= 4) {
        f32x4 qc[3] = {z4, z4, z4};
        const int j = w & 3;
#pragma unroll
        for (int i = 0; i < 3; ++i) {
          const int kb = j + 4 * i, o = kb < 5 ? 4 * KA_SLOT + kb * 5120 : (kb - 5) * 5120;
          if (i < 2 || j == 0) {
#pragma unroll
            for (int h = 0; h < 2; ++h) {
              const lptr q0 = bqp[0] + o + h * 2560, q1 = bq[0] + o + h * 2560;
              qc[0] = MFMA16(cat8(trd(q0), trd(q0 + 1280)), ones, qc[0]);
              qc[1] = MFMA16(cat8(trd(q0 + 32), trd(q0 + 1280 + 32)), ones, qc[1]);
              qc[2] = MFMA16(cat8(trd(q1), trd(q1 + 1280)), ones, qc[2]);
            }
          }
        }
        if (fr == 0) {
#pragma unroll
          for (int i = 0; i < 3; ++i) *(LAS f32x4*)(L + KA_QC + ((i * 4 + fq) * 4 + j) * 16) = qc[i];
        }
        asm volatile("s_waitcnt vmcnt(0) lgkmcnt(0)\n\ts_barrier" ::: "memory");
        {
          const unsigned vo0 = mko(j, false), vo1 = mko(j + 4, false), vo8 = mko(8, false) + j * 144 * 1152, wj = lds0 + j * 1024;
          static_for<0, 8>([&](auto bc) AI {
            constexpr int b = decltype(bc)::v;
            dma<KB_WO + b * 9216, b * 72 * 1152>(rO, vo0, wj);
            dma<KB_WO + b * 9216 + 4096, b * 72 * 1152>(rO, vo1, wj);
          });
          dma<KB_WO + 8192, 0>(rO, vo8, lds0 + j * 18432);
          dma<KB_WO + 9216 + 8192, 72 * 1152>(rO, vo8, lds0 + j * 18432);
          dma<KB_WO + 8 * 9216, 0>(rO, mko(j, true), wj);
          dma<KB_WO + 8 * 9216 + 4096, 0>(rO, mko(j + 4, true), wj);
          if (j == 0) dma<KB_WO + 8 * 9216 + 8192, 0>(rO, mko(8, true), lds0);
        }
      }
      rq(0); rq(1);
      static_for<0, 18>([&](auto ic) AI {
        constexpr int s = decltype(ic)::v;
        if constexpr (s < 16) rq(s + 2);
#pragma unroll
        for (int i = 0; i < 3; ++i) qa[i] = MFMA16(b[s % 3][i], a[s], qa[i]);
        SBAR();
        if constexpr (s == PUBSTEP) {
          if (w < 4) {
            asm volatile("s_waitcnt vmcnt(0)\n\ts_barrier" ::: "memory");
            if (tid == 0) st8_dev(rW, WS_F1 + (p * 16 + tt) * 64, (u32x2){tag0, tag1});
            STAMP(6);
          }
        }
      });
      STAMP(7);
      u32x4 v[16];
      if (w < 4) {
        poll(rW, WS_F1 + (p * 16 + fr) * 64, tag0, tag1, 0);
        STAMP(8);
        const unsigned vo = recb + min(tid, 205) * 16;
#pragma unroll
        for (int t = 0; t < 16; ++t) asm volatile("buffer_load_dwordx4 %0, %1, %2, %3 offen sc1" : "=v"(v[t]) : "v"(vo), "s"(rW), "s"(t * REC) : "memory");
      }
      u32x4 pqf0;
      u32x2 pqf1;
      {
        float pq[3][4];
#pragma unroll
        for (int i = 0; i < 3; ++i) {
          const lptr qp = L + KA_QC + (i * 4 + fq) * 64;
          const f32x4 cq = (*(LAS f32x4*)qp + *(LAS f32x4*)(qp + 16)) + (*(LAS f32x4*)(qp + 32) + *(LAS f32x4*)(qp + 48));
#pragma unroll
          for (int k = 0; k < 4; ++k) pq[i][k] = phi_f((qa[i][k] - meanT * cq[k]) * rstdT);
        }
        pqf0 = (u32x4){pk2(pq[0][0], pq[0][1]), pk2(pq[0][2], pq[0][3]), pk2(pq[1][0], pq[1][1]), pk2(pq[1][2], pq[1][3])};
        pqf1 = (u32x2){fq == 0 ? pk2(pq[2][0], pq[2][1]) : 0u, fq == 0 ? pk2(pq[2][2], pq[2][3]) : 0u};
        PIN(pqf0); PIN(pqf1);
      }
      STAMP(9);
      if (w < 4) {
        asm volatile("s_waitcnt vmcnt(0)"
                     : "+v"(v[0]), "+v"(v[1]), "+v"(v[2]), "+v"(v[3]), "+v"(v[4]), "+v"(v[5]), "+v"(v[6]), "+v"(v[7]), "+v"(v[8]), "+v"(v[9]),
                       "+v"(v[10]), "+v"(v[11]), "+v"(v[12]), "+v"(v[13]), "+v"(v[14]), "+v"(v[15])::"memory");
#define BC(i) __builtin_bit_cast(f32x4, v[i])
        const f32x4 s = (((BC(0) + BC(1)) + (BC(2) + BC(3))) + ((BC(4) + BC(5)) + (BC(6) + BC(7)))) +
                        (((BC(8) + BC(9)) + (BC(10) + BC(11))) + ((BC(12) + BC(13)) + (BC(14) + BC(15))));
#undef BC
        const unsigned cm = tab[1] >> 24;
        const lptr cd = L + (tab[1] & 0xffffff);
        if (cm < 32) {
          *(LAS u32x2*)cd = (u32x2){pk2((cm & 1) ? s[0] : 0.f, (cm & 2) ? s[1] : 0.f), pk2((cm & 4) ? s[2] : 0.f, (cm & 8) ? s[3] : 0.f)};
          if (cm >= 16)
            *(LAS u32x2*)(cd + 96) = (u32x2){pk2((cm & 1) ? 0.f : s[0], (cm & 2) ? 0.f : s[1]), pk2((cm & 4) ? 0.f : s[2], (cm & 8) ? 0.f : s[3])};
        } else if (cm == 32) {
          *(LAS f32x4*)cd = s * (1.f / 576.f);
        }
      }
      lds_bar();
      STAMP(10);
      {
        const lptr kr = L + KA_KVI + fr * 96 + fq * 8;
        const bf16x8 b0 = __builtin_bit_cast(bf16x8, pqf0), b1 = __builtin_bit_cast(bf16x8, ((u32x4){pqf1[0], pqf1[1], 0u, 0u}));
        f32x4 T[3], rm[3];
#pragma unroll
        for (int i = 0; i < 3; ++i) {
          const u32x2 a0 = *(LAS u32x2*)(kr + i * 1536), a1 = *(LAS u32x2*)(kr + i * 1536 + 32), a2 = *(LAS u32x2*)(kr + i * 1536 + 64);
          rm[i] = *(LAS f32x4*)(L + KA_RS + (i < 2 ? 16 * i + 4 * fq : 32) * 4);
          T[i] = MFMA16(__builtin_bit_cast(bf16x8, ((u32x4){a0[0], a0[1], a1[0], a1[1]})), b0, z4);
          T[i] = MFMA16(__builtin_bit_cast(bf16x8, ((u32x4){a2[0], a2[1], 0u, 0u})), b1, T[i]);
        }
        const float d0 = sum_fq(fq == 1 ? T[2][0] : 0.f), d1 = sum_fq(fq == 1 ? T[2][1] : 0.f);
        const float i0 = __builtin_amdgcn_rcpf(d0 + 1e-6f), i1 = __builtin_amdgcn_rcpf(d1 + 1e-6f), il = fq == 0 ? i0 : i1;
        const u32x4 om = {pk2(T[0][0] * i0, T[0][1] * i0), pk2(T[0][2] * i0, T[0][3] * i0), pk2(T[1][0] * il, T[1][1] * il), pk2(T[1][2] * i1, T[1][3] * i1)};
        const u32x2 ol = {pk2(T[2][0] * i1, T[2][1] * i1), pk2(T[2][2] * i1, T[2][3] * i1)};
        const unsigned afb = WS_AF + (tok0 >> 6) * 18 * 4096 + ((tok0 >> 4) & 3) * 1024;
        st16_dev(rW, afb + p * 4096 + lane * 16, om);
        if (fq == 0) st8_dev(rW, afb + (16 + (p >> 3)) * 4096 + (fr + 16 * (p & 3)) * 16 + ((p >> 2) & 1) * 8, ol);
        float pm = ((bflo(om[0]) * rm[0][0] + bfhi(om[0]) * rm[0][1]) + (bflo(om[1]) * rm[0][2] + bfhi(om[1]) * rm[0][3])) +
                   ((bflo(om[2]) * rm[1][0] + bfhi(om[2]) * rm[1][1]) + (bflo(om[3]) * rm[1][2] + bfhi(om[3]) * rm[1][3]));
        const float pl = (bflo(ol[0]) * rm[2][0] + bfhi(ol[0]) * rm[2][1]) + (bflo(ol[1]) * rm[2][2] + bfhi(ol[1]) * rm[2][3]);
        pm = sum_fq(pm + (fq == 0 ? pl : 0.f)) + (p == 0 ? meanT : 0.f);
        if (fq == 0) st4_dev(rW, WS_PM + p * 8192 + (tok0 + fr) * 4, __float_as_uint(pm));
      }
    }
    STAMP(11);
    asm volatile("s_waitcnt vmcnt(0) lgkmcnt(0)\n\ts_barrier" ::: "memory");
    if (tid == 0) st8_dev(rW, WS_F2 + (p * 16 + tt) * 64, (u32x2){tag0, tag1});
    STAMP(12);
  }

  {
    const int t64 = (xcd & 3) * 8 + (r & 7), j = (xcd >> 2) * 4 + (r >> 3);
    const int msub = w & 3, kh = w >> 2;
    const int tok = t64 * 64 + msub * 16 + fr;
    u32x2 xv[3];
    {
      const char* xr = (const char*)x + (size_t)tok * 1152 + 144 * j;
      xv[0] = *(const u32x2*)(xr + kh * 64 + 8 * fq);
      xv[1] = *(const u32x2*)(xr + kh * 64 + 32 + 8 * fq);
      xv[2] = *(const u32x2*)(xr + 128 + 8 * (fq & 1));
    }
    bf16x8 bv[9][5];
    {
      lptr pb[2], p4[2];
      pb[0] = L + KB_WO + (2 * (4 * (fq & 1) + q4) + (fq >> 1)) * 144 + kh * 4608 + 8 * pp;
      p4[0] = pb[0] - 8 * pp + 128 + 8 * (pp & 1);
      pb[1] = pb[0] + 5 * 9216; p4[1] = p4[0] + 5 * 9216;
      PIN(pb[1]); PIN(p4[1]);
#pragma unroll
      for (int kb = 0; kb < 9; ++kb) {
        const int h = kb >= 5, o = (kb - 5 * h) * 9216;
#pragma unroll
        for (int i = 0; i < 4; ++i) bv[kb][i] = cat8(trd(pb[h] + o + 32 * i), trd(pb[h] + o + 32 * i + 2304));
        bv[kb][4] = cat8(trd(p4[h] + o), trd(p4[h] + o + 2304));
      }
#pragma unroll
      for (int kb = 0; kb < 9; ++kb)
#pragma unroll
        for (int i = 0; i < 5; ++i) PIN(bv[kb][i]);
    }
    poll(rW, WS_F2 + (fr * 16 + (t64 >> 1)) * 64, tag0, tag1, 0);
    if (blockIdx.x == 0 && tid == 0) st8_dev(rW, WS_EPOCH, (u32x2){tag0, tag1});
    STAMP(13);
    u32x4 av[9];
    {
      const unsigned va = WS_AF + t64 * 18 * 4096 + kh * 4096 + msub * 1024 + lane * 16;
#pragma unroll
      for (int kb = 0; kb < 9; ++kb) av[kb] = ld16_dev(rW, va, kb * 8192);
    }
    float pmv[4];
#pragma unroll
    for (int i = 0; i < 4; ++i) pmv[i] = __uint_as_float(ld4_dev(rW, WS_PM + (4 * fq + i) * 8192 + tok * 4));
    SBAR();
    f32x4 acc[5];
#pragma unroll
    for (int i = 0; i < 5; ++i) acc[i] = z4;
#pragma unroll
    for (int kb = 0; kb < 9; ++kb)
#pragma unroll
      for (int i = 0; i < 5; ++i) acc[i] = MFMA16(bv[kb][i], __builtin_bit_cast(bf16x8, av[kb]), acc[i]);
    const float mean2 = sum_fq((pmv[0] + pmv[1]) + (pmv[2] + pmv[3]));
    STAMP(14);
    STAMP(15);
    {
      const lptr xs = L + 106496 + (msub * 2 + kh) * 3072 + lane * 16;
      *(LAS f32x4*)(xs) = kh == 0 ? acc[2] : acc[0];
      *(LAS f32x4*)(xs + 1024) = kh == 0 ? acc[3] : acc[1];
      *(LAS f32x4*)(xs + 2048) = acc[4];
    }
    lds_bar();
    f32x4 o[3];
    {
      const lptr xs = L + 106496 + (msub * 2 + (1 - kh)) * 3072 + lane * 16;
      o[0] = (kh == 0 ? acc[0] : acc[2]) + *(LAS f32x4*)(xs);
      o[1] = (kh == 0 ? acc[1] : acc[3]) + *(LAS f32x4*)(xs + 1024);
      o[2] = acc[4] + *(LAS f32x4*)(xs + 2048);
    }
    u32x2 pk[3];
    float ssq = 0.f;
#pragma unroll
    for (int i = 0; i < 3; ++i) {
      pk[i] = (u32x2){pk2(bflo(xv[i][0]) + o[i][0] - mean2, bfhi(xv[i][0]) + o[i][1] - mean2),
                      pk2(bflo(xv[i][1]) + o[i][2] - mean2, bfhi(xv[i][1]) + o[i][3] - mean2)};
      const float a0 = bflo(pk[i][0]), a1 = bfhi(pk[i][0]), a2 = bflo(pk[i][1]), a3 = bfhi(pk[i][1]);
      const float q = (a0 * a0 + a1 * a1) + (a2 * a2 + a3 * a3);
      ssq += (i < 2 || (kh == 0 && fq < 2)) ? q : 0.f;
    }
    ssq = sum_fq(ssq);
    const unsigned hb = WS_HCF + ((tok >> 7) * 18 * 8 + ((tok >> 4) & 7)) * 1024;
    st16_dev(rW, hb + (2 * j + kh) * 8192 + lane * 16, (u32x4){pk[0][0], pk[0][1], pk[1][0], pk[1][1]});
    if (kh == 0 && fq < 2) st8_dev(rW, hb + (16 + (j >> 2)) * 8192 + (fr + 16 * (j & 3)) * 16 + fq * 8, pk[2]);
    if (fq == 0) st4_dev(rW, WS_SSQ + (2 * j + kh) * 8192 + tok * 4, __float_as_uint(ssq));
    if (j == 0 && kh == 0 && fq == 1) st4_dev(rW, WS_MEAN2 + tok * 4, __float_as_uint(mean2));
    STAMP(16);
    asm volatile("s_waitcnt vmcnt(0) lgkmcnt(0)\n\ts_barrier" ::: "memory");
    if (tid == 0) st8_dev(rW, WS_F3 + (t64 * 8 + j) * 64, (u32x2){tag0, tag1});
    STAMP(17);
  }

  const int fs = xcd * 2 + (r & 1), mh = r >> 1;
  const int hh = fs & 1, t64o = mh * 2 + hh;
  {
    const rsrc_t rD = mkrs(wd, 576 * 4608 * 2);
    const int wm = w & 1, wn = w >> 1;
    const bool lo = w < 4;
    unsigned vb0, vb1, vbx;
    auto mkb = [&](int pi, bool left) AI {
      const int c = pi * 64 + lane, rho = c / 36, cs = c - rho * 36;
      const int row = left ? 72 * ((rho >> 2) & 3) + 64 + 4 * (rho >> 4) + (rho & 3) : rho;
      return (unsigned)(row * 9216 + ((((cs >> 1) ^ ((rho >> 2) & 1)) << 1) + (cs & 1)) * 16 + fs * 576);
    };
    vb0 = mkb(w, false); vb1 = mkb(w + 8, false); vbx = mkb(16 + (w & 1), false);
    const rsrc_t rXt = w < 2 ? rG : rU;
    const unsigned wlx = lds0 + (w < 2 ? F_G : F_U) + (16 + (w & 1)) * 1024;
    const unsigned vA = WS_HCF + mh * 18 * 8192 + tid * 16;
    const unsigned vd = (unsigned)((fs * 288 + w * 8 + (lane >> 3)) * 1152 + (((((lane & 7) >> 1) ^ (fq & 3)) << 1) + (lane & 1)) * 16);
    const unsigned vd2 = vd + (hh ? -288 * 1152 : 288 * 1152);
    auto p1 = [&](auto sc, int i) AI {
      constexpr int s = decltype(sc)::v, so = (s % 3) * F_STG;
      constexpr int go = (s < 16 ? 72 * (s >> 1) + 32 * (s & 1) : 288 * (s - 16)) * 9216;
      if ((ABL == 30 || ABL == 33 || ABL == 34) && s >= 3) return;
      if (i == 0) dma<so + F_G, go>(rG, vb0, wl1);
      if (i == 1) dma<so + F_U, go>(rU, vb0, wl1);
      if (i == 2) dma_dev<so, s * 8192>(rW, vA, wl1);
      if (i == 3) dma<so + F_G + 8192, go>(rG, vb1, wl1);
      if (i == 4) dma<so + F_U + 8192, go>(rU, vb1, wl1);
      if (i == 5) dma<so, go>(rXt, vbx, wlx);
    };
    auto p2 = [&](auto cc, auto ic) AI {
      constexpr int c = decltype(cc)::v, i = decltype(ic)::v;
      dma<f_wslot(f_reg(c)) + i * 8192, (c % 9) * 128 + i * 64 * 1152>(rD, c < 9 ? vd : vd2, wl1);
    };
    auto slot = [&](auto stc, auto slc) AI {
      constexpr int st = decltype(stc)::v, sl = decltype(slc)::v;
      if constexpr (st < 18) {
        constexpr int pl = sl == 0 ? 0 : sl == 2 ? 1 : sl == 3 ? 2 : sl == 5 ? 3 : sl == 6 ? 4 : sl == 8 ? 5 : -1;
        constexpr int ph = sl == 1 ? 0 : sl == 2 ? 1 : sl == 4 ? 2 : sl == 5 ? 3 : sl == 7 ? 4 : -1;
        if constexpr (pl >= 0 && pl == ph) p1(stc, pl);
        else if constexpr (pl >= 0) { if (lo) p1(stc, pl); }
        else if constexpr (ph >= 0) { if (!lo) p1(stc, ph); }
      } else if constexpr (st < 21) {
        if constexpr ((sl & 1) == 0) { if (lo) p2(IC<st - 18>{}, IC<(sl >> 1)>{}); }
        else { if (!lo) p2(IC<st - 18>{}, IC<(sl >> 1)>{}); }
      }
    };
    auto wts = [&]() AI {
      static_for<0, 3>([&](auto sc) AI {
        p1(sc, 0); p1(sc, 1); p1(sc, 3); p1(sc, 4);
        if (lo) p1(sc, 5);
      });
    };
    if (w == (mh & 7)) { poll(rW, WS_F3 + (mh * 16 + fr) * 64, tag0, tag1, 0); asm volatile("s_barrier" ::: "memory"); wts(); }
    else { wts(); asm volatile("s_barrier" ::: "memory"); }
    STAMP(18);
    dma_dev<F_SSQ, 0>(rW, (unsigned)(WS_SSQ + (2 * w + (lane >> 5)) * 8192 + mh * 512 + (lane & 31) * 16), wl1);
    p1(IC<0>{}, 2); p1(IC<1>{}, 2); p1(IC<2>{}, 2);
    const int gb = fq & 1;
    const int rowb = (4 * fq + q4) * 576;
    lptr pE[3], pO[3], pX[3], pA[2];
    pE[0] = L + F_G + rowb + 128 * wn + 32 * gb + 8 * pp;
    pO[0] = L + F_G + rowb + 128 * wn - 32 * gb + 8 * pp;
    pX[0] = L + rowb + (pp < 2 ? F_G : F_U) + ((16 + (wn >> 1)) ^ gb) * 32 + 16 * (wn & 1) + 8 * (pp & 1);
    pA[0] = L + (wm * 4) * 1024 + lane * 16;
    pE[1] = pE[0] + F_STG; pO[1] = pO[0] + F_STG; pE[2] = pE[0] + 2 * F_STG; pO[2] = pO[0] + 2 * F_STG;
    pX[1] = pX[0]; pX[2] = pX[0] + 2 * F_STG; pA[1] = pA[0] + 2 * F_STG;
    PIN(pE[1]); PIN(pO[1]); PIN(pE[2]); PIN(pO[2]); PIN(pX[2]); PIN(pA[1]);

    f32x4 acc[9][4];
    bf16x8 A[2][4], B[9];
    auto rdA = [&](int s, int m) AI { if ((ABL == 31 || ABL == 33 || ABL == 34) && s >= 2) return; A[s & 1][m] = ldfrag((s % 3 == 2 ? pA[1] : pA[0] + (s % 3) * F_STG) + m * 1024); };
    auto rdB = [&](int s, int n) AI {
      if ((ABL == 31 || ABL == 33 || ABL == 34) && s >= 1) return;
      const int b = s % 3;
      if (n < 8) {
        const int nn = n >> 1, off = ((n & 1) ? F_U - F_G : 0) + 32 * nn;
        const lptr q = (nn & 1) ? pO[b] : pE[b];
        B[n] = cat8(trd(q + off), trd(q + off + 16 * 576));
      } else {
        const lptr q = pX[b] + (b == 1 ? F_STG : 0);
        B[n] = cat8(trd(q), trd(q + 16 * 576));
      }
    };
    auto grh = [&](int s, int n, int h) AI {
#pragma unroll
      for (int m = 2 * h; m < 2 * h + 2; ++m) {
        if (s == 0) MFMA_NEW(acc[n][m], B[n], A[s & 1][m]); else MFMA_ACC(acc[n][m], B[n], A[s & 1][m]);
      }
      SBAR();
    };
    wait_bar<(B2POS == 2 && WSCH == 0 ? 4 : 2)>();
    STAMP(19);
#pragma unroll
    for (int m = 0; m < 4; ++m) rdA(0, m);
#pragma unroll
    for (int n = 0; n < 8; ++n) rdB(0, n);
    if (tid < 128) {
      float s = 0.f;
#pragma unroll
      for (int i = 0; i < 16; ++i) s += *(LAS float*)(L + F_SSQ + i * 512 + tid * 4);
      *(LAS float*)(L + F_RS + tid * 4) = __builtin_amdgcn_rsqf(s * (1.f / 576.f) + 1e-5f);
    }
    SBAR();
    if (w >= 4) __builtin_amdgcn_s_setprio(1);
    float rs[4];
    static_for<0, 18>([&](auto ic) AI {
      constexpr int s = decltype(ic)::v;
      constexpr bool nx = s < 17;
      using SP = IC<(s >= 1 ? s + 2 : 99)>;
      using SN = IC<s + 3>;
      grh(s, 0, 0); slot(SP{}, IC<6>{}); rdB(s, 8); SBAR(); grh(s, 0, 1);
      grh(s, 1, 0); slot(SP{}, IC<7>{}); SBAR(); grh(s, 1, 1);
      grh(s, 2, 0); slot(SP{}, IC<8>{}); SBAR(); grh(s, 2, 1);
      if ((ABL == 32 || ABL == 34) && s >= 2 && s <= 14) {} else
      if (s == 0) wait_bar<1>(); else if (s <= 15) wait_bar<6>(); else if (s == 16) wait_bar<5>(); else lds_bar();
      if constexpr (s <= 8) {
        const int li = (s * 128 + (gw >> 1)) * 3 + min(lane, 2), row = li / 9;
        const unsigned off = (unsigned)((216 + row) * 9216 + (li - row * 9) * 128 + xcd * 1152);
        if (w & 1) touch<F_RS + 1024>(rU, off, lds0 + w * 256); else touch<F_RS + 1024>(rG, off, lds0 + w * 256);
        SBAR();
      } else if constexpr (s <= 15) {
        touch<F_RS + 1024>(rD, (unsigned)(xcd * 5184 + min(((s - 9) * 256 + gw) * 3 + min(lane, 2), 5183)) * 128, lds0 + w * 256);
        SBAR();
      }
      if constexpr (s == 13) { vb0 = mkb(w, true); vb1 = mkb(w + 8, true); vbx = mkb(16 + (w & 1), true); SBAR(); }
      grh(s, 3, 0); slot(SN{}, IC<0>{}); if (nx) { rdA(s + 1, 0); rdA(s + 1, 1); rdB(s + 1, 0); } SBAR(); grh(s, 3, 1);
      grh(s, 4, 0); slot(SN{}, IC<1>{}); if (nx) { rdA(s + 1, 2); rdA(s + 1, 3); rdB(s + 1, 1); } SBAR(); grh(s, 4, 1);
      grh(s, 5, 0); slot(SN{}, IC<2>{}); if (nx) { rdB(s + 1, 2); rdB(s + 1, 3); } SBAR(); grh(s, 5, 1);
      grh(s, 6, 0); slot(SN{}, IC<3>{}); if (nx) { rdB(s + 1, 4); rdB(s + 1, 5); } SBAR(); grh(s, 6, 1);
      grh(s, 7, 0); slot(SN{}, IC<4>{}); if (nx) { rdB(s + 1, 6); }
      if (s == 16) {
#pragma unroll
        for (int m = 0; m < 4; ++m) rs[m] = *(LAS float*)(L + F_RS + (wm * 64 + m * 16 + fr) * 4);
      }
      SBAR(); grh(s, 7, 1);
      grh(s, 8, 0); slot(SN{}, IC<5>{}); SBAR(); grh(s, 8, 1); if (nx) { rdB(s + 1, 7); } SBAR();
    });
    asm volatile("s_nop 7\n\ts_nop 1" ::: "memory");
    SBAR();
    STAMP(20);
    slot(IC<20>{}, IC<6>{}); slot(IC<20>{}, IC<7>{}); slot(IC<20>{}, IC<8>{});
    {
      const bool own = wm == hh;
      const lptr hb = L + F_H;
      const unsigned hx = WS_HX + (mh * 16 + fs) * 36864;
      auto glu = [&](float g, float u, float rsv, float rnv) AI { return __builtin_amdgcn_rcpf(1.f + __builtin_amdgcn_exp2f(g * rnv)) * (u * rsv); };
#pragma unroll
      for (int pr = 0; pr < 2; ++pr) {
#pragma unroll
        for (int m = 0; m < 4; ++m) {
          const float rsv = rs[m], rnv = rsv * -1.4426950408889634f;
          const f32x4 g0 = acc[4 * pr][m], u0 = acc[4 * pr + 1][m], g1 = acc[4 * pr + 2][m], u1 = acc[4 * pr + 3][m];
          u32x4 o;
          o[0] = pk2(glu(g0[0], u0[0], rsv, rnv), glu(g0[1], u0[1], rsv, rnv));
          o[1] = pk2(glu(g0[2], u0[2], rsv, rnv), glu(g0[3], u0[3], rsv, rnv));
          o[2] = pk2(glu(g1[0], u1[0], rsv, rnv), glu(g1[1], u1[1], rsv, rnv));
          o[3] = pk2(glu(g1[2], u1[2], rsv, rnv), glu(g1[3], u1[3], rsv, rnv));
          const int off = (2 * wn + pr) * 4096 + m * 1024 + lane * 16;
          if (own) *(LAS u32x4*)(hb + off) = o; else st16_dev(rW, hx + off, o);
        }
      }
      const bool up = lane >= 32;
#pragma unroll
      for (int mp = 0; mp < 2; ++mp) {
        float g[4], u[4];
#pragma unroll
        for (int rr = 0; rr < 4; ++rr) {
          auto sw = __builtin_amdgcn_permlane32_swap(__float_as_uint(acc[8][2 * mp][rr]), __float_as_uint(acc[8][2 * mp + 1][rr]), false, false);
          g[rr] = __uint_as_float(sw[0]);
          u[rr] = __uint_as_float(sw[1]);
        }
        const float rsv = up ? rs[2 * mp + 1] : rs[2 * mp], rnv = rsv * -1.4426950408889634f;
        const int m = 2 * mp + (up ? 1 : 0);
        const int gam = 2 * wn + gb;
        const int off = 8 * 4096 + m * 1024 + (fr + 16 * (gam & 3)) * 16 + (gam >> 2) * 8;
        const u32x2 o = {pk2(glu(g[0], u[0], rsv, rnv), glu(g[1], u[1], rsv, rnv)), pk2(glu(g[2], u[2], rsv, rnv), glu(g[3], u[3], rsv, rnv))};
        if (own) *(LAS u32x2*)(hb + off) = o; else st8_dev(rW, hx + off, o);
      }
    }
    STAMP(21);
    wait_bar<8>();
    lptr pD[4];
    {
      const int rho = 4 * fq + q4;
      pD[0] = L + rho * 128 + ((wn ^ ((rho >> 1) & 3)) * 32) + 8 * pp;
      pD[1] = pD[0] + f_wslot(1); pD[2] = pD[0] + f_wslot(2); pD[3] = pD[0] + f_wslot(3);
      PIN(pD[1]); PIN(pD[2]); PIN(pD[3]);
    }
    auto rdD = [&](int c, int k) AI { if (ABL == 22 && c > 0) return; B[k] = cat8(trd(pD[f_reg(c)] + k * 4096), trd(pD[f_reg(c)] + k * 4096 + 2048)); };
    bf16x8 HA[9][2];
    lptr hr = L + F_H + wm * 2048 + lane * 16;
    auto rdH = [&](int k) AI { HA[k][0] = ldfrag(hr + k * 4096); HA[k][1] = ldfrag(hr + k * 4096 + 1024); };
#pragma unroll
    for (int k = 0; k < 9; ++k) { if (k < 8) rdD(0, k); rdH(k); }
    hr -= F_H;
    const unsigned part = WS_PART + (((fs >> 1) * 32 + t64o) * 72 + w) * 1024 + lane * 16;
    const unsigned f4 = WS_F4 + (t64o * 8 + (fs >> 1)) * 64;
    const unsigned vhx = WS_HX + (mh * 16 + (fs ^ 1)) * 36864 + tid * 16;
    f32x4 o[9][2];
    auto st1 = [&](int cb) AI {
      const f32x4 a = o[cb][0], b = o[cb][1];
      __builtin_amdgcn_raw_buffer_store_b128((u32x4){pk2(a[0], a[1]), pk2(a[2], a[3]), pk2(b[0], b[1]), pk2(b[2], b[3])}, rW, part, cb * 8192, 16);
    };
    auto gd = [&](int c, int k) AI {
#pragma unroll
      for (int m = 0; m < 2; ++m) {
        if (ABL == 21 && k > 0) asm volatile("" :: "v"(B[k]), "v"(HA[k][m]));
        else o[c % 9][m] = MFMA16(B[k], HA[k][m], (c < 9 && k == 0) ? z4 : o[c % 9][m]);
      }
      SBAR();
    };
    SBAR();
    const unsigned f5p = WS_F5 + (mh * 16 + (fs ^ 1)) * 64;
    u32x2 pf;
    auto pcs = [&](auto stc, auto nc) AI {
      constexpr int st = decltype(stc)::v;
      static_for<0, decltype(nc)::v>([&](auto ic) AI {
        constexpr int i = decltype(ic)::v;
        if constexpr (ABL == 20) { if constexpr (st == 18) dma_dev<i * 8192, i * 8192>(rW, vhx, wl1); }
        else if constexpr (st < 18) p2(stc, ic); else dma_dev<i * 8192, i * 8192>(rW, vhx, wl1);
      });
    };
    auto slot2 = [&](auto ivc, auto slc) AI {
      constexpr int iv = decltype(ivc)::v, sl = decltype(slc)::v;
      constexpr int s1 = iv == 0 ? 3 : iv <= 3 ? iv + 4 : iv == 4 ? 18 : iv <= 9 ? iv + 3 : iv + 4;
      if constexpr (iv >= 0 && iv <= 13) {
        if constexpr (sl == LOSL) { if (lo) { pcs(IC<s1>{}, IC<5>{}); if constexpr (iv == 0 || iv == 9) pcs(IC<s1 + 1>{}, IC<5>{}); } }
        if constexpr (sl == HISL) { if (!lo) { pcs(IC<s1>{}, IC<4>{}); if constexpr (iv == 0 || iv == 9) pcs(IC<s1 + 1>{}, IC<4>{}); } }
      }
    };
    static_for<0, 18>([&](auto ic) AI {
      constexpr int c = decltype(ic)::v;
      constexpr bool nx = c < 17;
      using P = IC<c - 1>;
      gd(c, 0); slot2(P{}, IC<6>{}); rdD(c, 8); if constexpr (c == 8) rdH(0); if constexpr (c == 9) rdH(8); SBAR();
      gd(c, 1); slot2(P{}, IC<7>{}); if constexpr (c == 8) rdH(1); SBAR();
      gd(c, 2); slot2(P{}, IC<8>{}); if constexpr (c == 8) rdH(2); if constexpr (c >= 10) st1(c - 10); SBAR();
      constexpr int N = c == 0 ? 4 : c == 3 ? 9 : c <= 6 ? 8 : c <= 9 ? 4 : c == 10 ? 9 : c <= 14 ? 10 : c == 15 ? 6 : 2;
      if constexpr (c == 4) asm volatile("s_waitcnt vmcnt(8) lgkmcnt(0)\n\ts_barrier" : "+v"(pf)::"memory");
      else wait_bar<N>();
      if constexpr (c == 0) STAMP(26);
      if constexpr (c == 3) STAMP(27);
      if constexpr (c == 4) STAMP(28);
      if constexpr (c == 8) STAMP(29);
      if constexpr (c == 12) STAMP(30);
      if constexpr (c == 16) STAMP(31);
      if constexpr (c == 1) { if (tid == 0) st8_dev(rW, WS_F5 + (mh * 16 + fs) * 64, (u32x2){tag0, tag1}); }
      if constexpr (c >= 12) { if (tid == 0) st8_dev(rW, f4, (u32x2){tag0 + (c - 11), tag1}); }
      gd(c, 3);
      if constexpr (c == 2) asm volatile("buffer_load_dwordx2 %0, %1, %2, 0 offen sc1" : "=v"(pf) : "v"(f5p), "s"(rW) : "memory");
      if constexpr (c == 4) { if (!__all(pf[0] == tag0 && pf[1] == tag1)) poll(rW, f5p, tag0, tag1, 0); }
      slot2(ic, IC<0>{}); if (nx) { rdD(c + 1, 0); rdD(c + 1, 1); } if constexpr (c == 8) rdH(3); SBAR();
      gd(c, 4); slot2(ic, IC<1>{}); if (nx) { rdD(c + 1, 2); rdD(c + 1, 3); } if constexpr (c == 8) rdH(4); SBAR();
      gd(c, 5); slot2(ic, IC<2>{}); if (nx) rdD(c + 1, 4); if constexpr (c == 8) rdH(5); SBAR();
      gd(c, 6); slot2(ic, IC<3>{}); if (nx) rdD(c + 1, 5); if constexpr (c == 8) rdH(6); SBAR();
      gd(c, 7); slot2(ic, IC<4>{}); if (nx) rdD(c + 1, 6); if constexpr (c == 8) rdH(7); SBAR();
      gd(c, 8); slot2(ic, IC<5>{}); if (nx) rdD(c + 1, 7); SBAR();
    });
    st1(8);
    STAMP(22);
    asm volatile("s_waitcnt vmcnt(0)\n\ts_barrier" ::: "memory");
    if (tid == 0) st8_dev(rW, f4, (u32x2){tag0 + 9, tag1});
    STAMP(23);
  }

  if (w < 6) {
    const int q = fs >> 1, wm = q & 1, wn = q >> 1, ms0 = hh * 4 + wm * 2;
    const int c0 = w < 3 ? 2 * w : w + 3;
    auto hoff = [&](int c) AI {
      const int cg = 4 * (c * 4 + wn) + fq, j = cg / 18, gl = cg - 18 * j;
      return (unsigned)(WS_HCF + ((mh * 18 + (gl < 16 ? 2 * j + (gl >> 3) : 16 + (j >> 2))) * 8 + ms0) * 1024 +
                        (fr + 16 * (gl < 16 ? (gl & 3) : (j & 3))) * 16 + (gl < 16 ? ((gl >> 2) & 1) : gl - 16) * 8);
    };
    const unsigned ho0 = hoff(c0), ho1 = hoff(c0 + 1);
    u32x2 hc[2][2];
    float mu[2];
#pragma unroll
    for (int i = 0; i < 2; ++i) {
      hc[0][i] = ld8_dev(rW, ho0 + i * 1024);
      hc[1][i] = ld8_dev(rW, ho1 + i * 1024);
      mu[i] = __uint_as_float(ld4_dev(rW, WS_MEAN2 + (mh * 128 + (ms0 + i) * 16 + fr) * 4));
    }
    poll(rW, WS_F4 + (t64o * 8 + (lane & 7)) * 64, tag0, tag1, w < 3 ? c0 + 2 : 9);
    STAMP(24);
    const unsigned vo = WS_PART + (t64o * 72 + q) * 1024 + c0 * 8192 + lane * 16;
    auto fin = [&](int b, const u32x4(&v)[8]) AI {
      float s[8];
#pragma unroll
      for (int k = 0; k < 8; ++k) s[k] = 0.f;
#pragma unroll
      for (int f = 0; f < 8; ++f)
#pragma unroll
        for (int k = 0; k < 4; ++k) { s[2 * k] += bflo(v[f][k]); s[2 * k + 1] += bfhi(v[f][k]); }
#pragma unroll
      for (int i = 0; i < 2; ++i) {
        const u32x2 o = {pk2(s[4 * i] + (bflo(hc[b][i][0]) + mu[i]), s[4 * i + 1] + (bfhi(hc[b][i][0]) + mu[i])),
                         pk2(s[4 * i + 2] + (bflo(hc[b][i][1]) + mu[i]), s[4 * i + 3] + (bfhi(hc[b][i][1]) + mu[i]))};
        *(u32x2*)(out + (size_t)(mh * 128 + (ms0 + i) * 16 + fr) * 576 + ((c0 + b) * 4 + wn) * 16 + 4 * fq) = o;
      }
    };
    u32x4 v0[8], v1[8];
#pragma unroll
    for (int f = 0; f < 8; ++f) v0[f] = ld16_dev(rW, vo, f * 2304 * 1024);
    if (w < 3) {
#pragma unroll
      for (int f = 0; f < 8; ++f) v1[f] = ld16_dev(rW, vo + 8192, f * 2304 * 1024);
    }
    fin(0, v0);
    if (w < 3) fin(1, v1);
    STAMP(25);
  }
}

extern "C" void kernel_launch(void* const* d_in, const int* in_sizes, int n_in, void* d_out, int out_size, void* d_ws, size_t ws_size,
                              hipStream_t stream) {
  mk_block<<<256, 512, 0, stream>>>((const u16*)d_in[0], (const u16*)d_in[1], (const u16*)d_in[2], (const u16*)d_in[4],
                                    (const u16*)d_in[5], (const u16*)d_in[6], (char*)d_ws, (u16*)d_out);
}
```

```cpp
#include <hip/hip_runtime.h>
#include <hip/hip_bf16.h>
#include <stdint.h>

constexpr int PUBSTEP = 5, LOSL = 0, HISL = 8;
typedef unsigned short u16;
typedef short bf16x8 __attribute__((ext_vector_type(8)));
typedef short s16x4 __attribute__((ext_vector_type(4)));
typedef float f32x4 __attribute__((ext_vector_type(4)));
typedef float f32x2 __attribute__((ext_vector_type(2)));
typedef unsigned u32x4 __attribute__((ext_vector_type(4)));
typedef unsigned u32x2 __attribute__((ext_vector_type(2)));
typedef __bf16 bf16x2_t __attribute__((ext_vector_type(2)));
typedef __amdgpu_buffer_rsrc_t rsrc_t;
#define LAS __attribute__((address_space(3)))
#define DI __device__ __forceinline__
#define AI __attribute__((always_inline))
typedef LAS char* lptr;

#define MFMA16(a, b, c) __builtin_amdgcn_mfma_f32_16x16x32_bf16((a), (b), (c), 0, 0, 0)
#define SBAR() __builtin_amdgcn_sched_barrier(0)
#define PIN(x) asm volatile("" : "+v"(x))
#define MFMA_ACC(c, a, b) asm("v_mfma_f32_16x16x32_bf16 %0, %1, %2, %0" : "+v"(c) : "v"(a), "v"(b))
#define MFMA_NEW(c, a, b) asm("v_mfma_f32_16x16x32_bf16 %0, %1, %2, 0" : "=&v"(c) : "v"(a), "v"(b))

constexpr int REC = 3328;
constexpr int WS_EPOCH = 0;
constexpr int WS_F1 = 256;
constexpr int WS_F2 = WS_F1 + 256 * 64;
constexpr int WS_F3 = WS_F2 + 256 * 64;
constexpr int WS_F4 = WS_F3 + 256 * 64;
constexpr int WS_F5 = WS_F4 + 256 * 64;
constexpr int WS_REC = WS_F5 + 256 * 64;
constexpr int WS_PM = WS_REC + 256 * REC;
constexpr int WS_AF = WS_PM + 16 * 8192;
constexpr int WS_HCF = WS_AF + 2048 * 576 * 2;
constexpr int WS_SSQ = WS_HCF + 2048 * 576 * 2;
constexpr int WS_MEAN2 = WS_SSQ + 16 * 8192;
constexpr int WS_HX = WS_MEAN2 + 8192;
constexpr int WS_PART = WS_HX + 256 * 36864;
constexpr int WS_END = WS_PART + 8 * 2048 * 576 * 2;

template <int I> struct IC { static constexpr int v = I; };
template <int I, int N, class Fn> DI void static_for(Fn&& f) {
  if constexpr (I < N) { f(IC<I>{}); static_for<I + 1, N>(f); }
}
DI unsigned pk2(float a, float b) {
  bf16x2_t v = {(__bf16)a, (__bf16)b};
  return __builtin_bit_cast(unsigned, v);
}
DI float bflo(unsigned w) { return __uint_as_float(w << 16); }
DI float bfhi(unsigned w) { return __uint_as_float(w & 0xffff0000u); }
DI s16x4 trd(lptr p) { return __builtin_bit_cast(s16x4, __builtin_amdgcn_ds_read_tr16_b64_v4i16((LAS s16x4*)p)); }
DI bf16x8 cat8(s16x4 lo, s16x4 hi) { return __builtin_shufflevector(lo, hi, 0, 1, 2, 3, 4, 5, 6, 7); }
DI bf16x8 ldfrag(lptr p) { return *(LAS bf16x8*)p; }
DI rsrc_t mkrs(const void* p, int bytes) { return __builtin_amdgcn_make_buffer_rsrc((void*)p, (short)0, bytes, 0x00020000); }
template <int LOFF, int GOFF> DI void dma(rsrc_t rs, unsigned voff, unsigned wl) {
  unsigned t;
  asm volatile("s_add_u32 m0, %3, %4\n\ts_mov_b32 %0, %5\n\tbuffer_load_dwordx4 %1, %2, %0 offen lds"
               : "=&s"(t) : "v"(voff), "s"(rs), "s"(wl), "i"(LOFF), "i"(GOFF) : "memory", "scc");
}
template <int LOFF, int GOFF> DI void dma_dev(rsrc_t rs, unsigned voff, unsigned wl) {
  unsigned t;
  asm volatile("s_add_u32 m0, %3, %4\n\ts_mov_b32 %0, %5\n\tbuffer_load_dwordx4 %1, %2, %0 offen sc1 lds"
               : "=&s"(t) : "v"(voff), "s"(rs), "s"(wl), "i"(LOFF), "i"(GOFF) : "memory", "scc");
}
template <int LOFF> DI void touch(rsrc_t rs, unsigned voff, unsigned wl) {
  asm volatile("s_add_u32 m0, %2, %3\n\ts_nop 0\n\tbuffer_load_dword %0, %1, 0 offen lds" ::"v"(voff), "s"(rs), "s"(wl), "i"(LOFF) : "memory", "scc");
}
template <int LOFF, int GOFF> DI void touch_o(rsrc_t rs, unsigned voff, unsigned wl) {
  unsigned t;
  asm volatile("s_add_u32 m0, %3, %4\n\ts_mov_b32 %0, %5\n\tbuffer_load_dword %1, %2, %0 offen lds"
               : "=&s"(t) : "v"(voff), "s"(rs), "s"(wl), "i"(LOFF), "i"(GOFF) : "memory", "scc");
}
DI void st16_dev(rsrc_t rs, unsigned voff, u32x4 v) { __builtin_amdgcn_raw_buffer_store_b128(v, rs, voff, 0, 16); }
DI void st8_dev(rsrc_t rs, unsigned voff, u32x2 v) { __builtin_amdgcn_raw_buffer_store_b64(v, rs, voff, 0, 16); }
DI void st4_dev(rsrc_t rs, unsigned voff, unsigned v) { __builtin_amdgcn_raw_buffer_store_b32(v, rs, voff, 0, 16); }
DI u32x4 ld16_dev(rsrc_t rs, unsigned voff, int soff) { return __builtin_amdgcn_raw_buffer_load_b128(rs, voff, soff, 16); }
DI u32x2 ld8_dev(rsrc_t rs, unsigned voff) { return __builtin_amdgcn_raw_buffer_load_b64(rs, voff, 0, 16); }
DI unsigned ld4_dev(rsrc_t rs, unsigned voff) { return __builtin_amdgcn_raw_buffer_load_b32(rs, voff, 0, 16); }
template <int N> DI void wait_bar() { asm volatile("s_waitcnt vmcnt(%0) lgkmcnt(0)\n\ts_barrier" ::"i"(N) : "memory"); }
DI void lds_bar() { asm volatile("s_waitcnt lgkmcnt(0)\n\ts_barrier" ::: "memory"); }
DI float phi_f(float t) { return (t > 0.f ? t + 1.f : __builtin_amdgcn_exp2f(t * 1.4426950408889634f)) + 1e-6f; }
DI float xor16(float v) { auto r = __builtin_amdgcn_permlane16_swap(__float_as_uint(v), __float_as_uint(v), false, false); return __uint_as_float(r[0]) + __uint_as_float(r[1]); }
DI float xor32(float v) { auto r = __builtin_amdgcn_permlane32_swap(__float_as_uint(v), __float_as_uint(v), false, false); return __uint_as_float(r[0]) + __uint_as_float(r[1]); }
DI float sum_fq(float v) { return xor32(xor16(v)); }
DI void poll(rsrc_t rs, unsigned voff, unsigned t0, unsigned t1, unsigned need) {
#pragma unroll 1
  for (unsigned spin = 0; spin < (1u << 17); ++spin) {
    u32x2 f;
    asm volatile("buffer_load_dwordx2 %0, %1, %2, 0 offen sc1\n\ts_waitcnt vmcnt(0)" : "=v"(f) : "v"(voff), "s"(rs) : "memory");
    const unsigned d = f[0] - t0;
    if (__all(f[1] == t1 && d >= need && d < 16u)) break;
    __builtin_amdgcn_s_sleep(1);
  }
}

#define STAMP_DECL
#define STAMP(i) do {} while (0)

constexpr int KA_SLOT = 16384 + 10240;
constexpr int KA_KVO = 16384;
constexpr int ka_q(int kb) { return kb < 5 ? 4 * KA_SLOT + kb * 5120 : (kb - 5) * 5120; }
constexpr int KA_EX = KA_SLOT;
constexpr int KB_WO = 20480;
constexpr int KA_WO = 5 * KA_SLOT;
constexpr int KA_CS = KA_WO + 3072;
constexpr int KA_KVI = KA_CS + 8 * 320;
constexpr int KA_RSP = KA_KVI + 6144;
constexpr int KA_ZZ = KA_RSP + 160;
constexpr int KA_RS = KA_ZZ + 160;
constexpr int KA_QC = KA_RS + 320;
constexpr int KA_DEAD = KA_QC + 768;
static_assert(KA_DEAD + 2048 <= 163840, "lds");

struct Tab { unsigned v[1024]; };
constexpr Tab make_tab() {
  Tab T{};
  for (int t = 0; t < 512; ++t) {
    const int w = t >> 6, ln = t & 63, fr = ln & 15, fq = ln >> 4;
    const int slot = w == 0 ? ln : w == 3 ? 64 + ln : w == 1 ? (fr < 2 ? 128 + fq * 2 + fr : -1) : w == 2 ? (fq == 0 ? 136 + fr : -1)
                   : w == 4 ? (fr == 8 ? 152 + fq : (ln < 8 ? 197 + ln : ln == 9 ? 205 : -1)) : w == 5 ? ((fr >= 4 && fr <= 8) ? 156 + fq * 5 + fr - 4 : -1)
                   : w == 6 ? (fq == 0 ? 176 + fr : -1) : ((fq == 0 && fr >= 4 && fr <= 8) ? 192 + fr - 4 : -1);
    int ce = 0, cd = 0;
    if (t < 128) { ce = (t & 15) + (t >> 6) * 16; cd = ((t >> 4) & 3) * 4 + (t >> 6) * 16; }
    else if (t < 136) { ce = 16 + (t & 1); cd = ((t - 128) >> 1) * 4; }
    else if (t < 152) { ce = t - 136; cd = 16; }
    else if (t < 156) { ce = 36; cd = (t - 152) * 4; }
    else if (t < 176) { const int i = t - 156, f = i / 5; ce = 32 + i - f * 5; cd = 16 + 4 * f; }
    else if (t < 192) { ce = t - 160; cd = 32; }
    else if (t < 197) { ce = 32 + t - 192; cd = 32; }
    int m = 0;
    for (int rr = 0; rr < 4; ++rr) m |= ((cd + rr >= 18) == (ce >= 18)) ? (1 << rr) : 0;
    int dst = KA_KVI + ce * 96 + cd * 2;
    if (ce == 36) m = 16 | (~m & 15);
    if (t >= 197) { dst = KA_RS + (t < 206 ? (t - 197) * 16 : 0); m = t < 206 ? 32 : 64; }
    T.v[2 * t] = slot >= 0 ? (unsigned)slot * 16u : 0x40000000u;
    T.v[2 * t + 1] = (unsigned)dst | ((unsigned)m << 24);
  }
  return T;
}
static __device__ const Tab g_tab = make_tab();

constexpr int F_G = 8192, F_U = 8192 + 18432 + 16, F_STG = F_U + 18432;
constexpr int F_SSQ = 3 * F_STG;
constexpr int F_RS = F_SSQ + 8192;
constexpr int F_H = 163840 - 73728;
constexpr int f_wslot(int i) { return i == 0 ? 0 : i == 1 ? F_STG : i == 2 ? F_H + 36864 : F_H; }
constexpr int f_reg(int c) { return c < 8 ? (c & 3) : c == 8 ? 1 : c == 9 ? 2 : c == 10 ? 3 : c == 11 ? 1 : c == 12 ? 2 : c == 13 ? 0 : c == 14 ? 3 : c == 15 ? 1 : c == 16 ? 2 : 0; }

__global__ __launch_bounds__(512, 2) void mk_block(const u16* __restrict__ x, const u16* __restrict__ wqkv, const u16* __restrict__ wo,
                                                   const u16* __restrict__ wg, const u16* __restrict__ wu, const u16* __restrict__ wd,
                                                   char* __restrict__ ws, u16* __restrict__ out) {
  __shared__ __attribute__((aligned(16))) char smem[163840];
  STAMP_DECL; STAMP(0);
  const lptr L = (lptr)smem;
  const unsigned lds0 = (unsigned)(uintptr_t)L;
  const int tid = threadIdx.x, lane = tid & 63, w = __builtin_amdgcn_readfirstlane(tid >> 6);
  const int fr = lane & 15, fq = lane >> 4, q4 = fr >> 2, pp = lane & 3;
  const int xcd = blockIdx.x & 7, r = blockIdx.x >> 3;
  const unsigned wl1 = lds0 + w * 1024, wl2 = lds0 + w * 2048;
  const rsrc_t rW = mkrs(ws, WS_END), rX = mkrs(x, 2048 * 576 * 2);
  const rsrc_t rG = mkrs(wg, 576 * 4608 * 2), rU = mkrs(wu, 576 * 4608 * 2);
  const f32x4 z4 = {0.f, 0.f, 0.f, 0.f};
  unsigned tag0, tag1;
  const int gw = r * 8 + w;

  const rsrc_t rO = mkrs(wo, 576 * 576 * 2);
  auto mko = [&](int pi, bool left) AI {
    const int c = pi * 64 + lane, row = c / 9, ch = c - row * 9, st = row >> 5, rho = row & 31, rr = rho & 15;
    const int t = rho >> 4, g = 2 * (rr & 1) + (rr >> 3), q = (rr >> 1) & 3;
    const int k = left ? 36 * (8 * st + 4 * t + g) + 32 + q : 36 * st + 16 * t + 4 * g + q;
    return (unsigned)(k * 1152 + ch * 16 + 144 * ((xcd >> 2) * 4 + (r >> 3)));
  };
  {
    const rsrc_t rQ = mkrs(wqkv, 576 * 1728 * 2);
    const int tt = (xcd & 3) * 4 + (r & 3), p = (xcd >> 2) * 8 + (r >> 2);
    const int o8 = (p & 1) * 8, ot = (tt & 1) * 8;
    const int tok0 = tt * 128 + w * 16;
    const unsigned vx = (unsigned)((tok0 + (lane >> 3)) * 1152 + (((lane & 7) ^ (lane >> 3)) * 16));
    {
      const unsigned g = blockIdx.x * 8 + w;
      unsigned long long pc;
      unsigned clen;
      asm volatile("s_getpc_b64 %0\n.Lmk_here:\n\ts_mov_b32 %1, .Lmk_end-.Lmk_here" : "=s"(pc), "=s"(clen));
      touch<KA_DEAD>(mkrs((const void*)pc, (int)clen), (unsigned)(gw * 2 + min(lane, 1)) * 128, lds0 + w * 256);
      touch<KA_DEAD>(rX, lane < 9 ? (g * 9 + lane) * 128 : 0x7fffff00u, lds0 + w * 256);
      touch<KA_DEAD>(rQ, lane < 8 ? (g * 8 + lane) * 128 : 0x7fffff00u, lds0 + w * 256);
      touch<KA_DEAD>(rO, lane < 3 ? (g * 3 + lane) * 128 : 0x7fffff00u, lds0 + w * 256);
    }
    dma<0, 0>(rX, vx, wl2);
    dma<1024, 8 * 1152>(rX, vx, wl2);
    auto mkv = [&](int pi) AI {
      const int c = pi * 64 + lane, row = c / 10, cc = c - row * 10, rho = row & 31;
      const int k = (row & 32) + ((rho >> 2) & 3) * 8 + (rho >> 4) * 4 + (rho & 3);
      return (unsigned)(k * 3456 + (cc >= 5 ? 1152 - 80 : 0) + cc * 16 + 1152 + 72 * p - o8);
    };
    const unsigned vkv0 = mkv(w), vkv8 = mkv(8 + (w & 1));
    const bool xtra = w >= 6;
    const unsigned wlx = lds0 + (w & 1) * 1024;
    auto piece = [&](auto kbc, int i) AI {
      constexpr int kb = decltype(kbc)::v, so = (kb % 5) * KA_SLOT;
      if (i == 0) dma<so, kb * 128>(rX, vx, wl2);
      if (i == 1) dma<so + 1024, kb * 128 + 8 * 1152>(rX, vx, wl2);
      if (i == 2) dma<so + KA_KVO, kb * 64 * 3456>(rQ, vkv0, wl1);
      if (i == 3) { if (xtra) dma<so + KA_KVO + 8192, kb * 64 * 3456>(rQ, vkv8, wlx); }
    };
    piece(IC<0>{}, 2); piece(IC<0>{}, 3);
    piece(IC<1>{}, 0); piece(IC<1>{}, 1); piece(IC<1>{}, 2); piece(IC<1>{}, 3);
    u32x2 ep, tab;
    asm volatile("buffer_load_dwordx2 %0, off, %1, 0 sc1" : "=v"(ep) : "s"(rW) : "memory");
    asm volatile("global_load_dwordx2 %0, %1, %2" : "=v"(tab) : "v"(tid * 8), "s"(g_tab.v) : "memory");
    if (w < 3) {
      const int c = min(tid, 179), rw = c / 5;
      dma<KA_WO, 0>(rO, (unsigned)((36 * p + rw) * 1152 + 72 * tt - ot + (c - rw * 5) * 16), wl1);
    }
    piece(IC<2>{}, 0); piece(IC<2>{}, 1); piece(IC<2>{}, 2); piece(IC<2>{}, 3);
    touch<KA_DEAD>(rQ, (unsigned)((gw * 7 + min(lane, 6)) * 1152 + 512), lds0 + w * 256);
    if (tid < 384) *(LAS u32x4*)(L + KA_KVI + tid * 16) = (u32x4){0u, 0u, 0u, 0u};
    unsigned vq;
    {
      const int c = (w < 5 ? w : 0) * 64 + lane, row = c / 5, ch = c - row * 5;
      const int rho = row & 31, rr = rho & 15;
      vq = (unsigned)(((row & 32) + 8 * (2 * (rr & 1) + (rr >> 3)) + 4 * (rho >> 4) + ((rr >> 1) & 3)) * 3456 + ch * 16 + 72 * p - o8);
    }
    lptr bk[3], bx[3], ba0[3], ba1[3];
    bk[0] = L + KA_KVO + (4 * fq + q4) * 160 + o8 + 8 * pp;
    bx[0] = L + KA_KVO + (4 * fq + q4) * 160 + 80 * (pp & 1) + o8 + 64;
    ba0[0] = L + w * 2048 + fr * 128 + (fq ^ (fr & 7)) * 16;
    ba1[0] = L + w * 2048 + fr * 128 + ((4 + fq) ^ (fr & 7)) * 16;
#pragma unroll
    for (int i = 1; i < 3; ++i) {
      bk[i] = bk[0] + 2 * i * KA_SLOT; bx[i] = bx[0] + 2 * i * KA_SLOT; ba0[i] = ba0[0] + 2 * i * KA_SLOT; ba1[i] = ba1[0] + 2 * i * KA_SLOT;
      PIN(bk[i]); PIN(bx[i]); PIN(ba0[i]); PIN(ba1[i]);
    }
    f32x4 acc[5], cs[5], gram = z4, sx = z4;
#pragma unroll
    for (int i = 0; i < 5; ++i) acc[i] = cs[i] = z4;
    bf16x8 ones = {0x3F80, 0x3F80, 0x3F80, 0x3F80, 0x3F80, 0x3F80, 0x3F80, 0x3F80};
    PIN(ones);
    bf16x8 a[18], B[2][5];
    auto readA = [&](int kb) AI {
      const int sl = kb % 5;
      a[2 * kb] = ldfrag(ba0[sl >> 1] + (sl & 1) * KA_SLOT);
      a[2 * kb + 1] = ldfrag(ba1[sl >> 1] + (sl & 1) * KA_SLOT);
    };
    auto readB = [&](int s, int i) AI {
      const int sl = (s >> 1) % 5, so = (sl & 1) * KA_SLOT + (s & 1) * 5120;
      if (i < 4) {
        const int off = so + (i >> 1) * 80 + 32 * (i & 1);
        B[s & 1][i] = cat8(trd(bk[sl >> 1] + off), trd(bk[sl >> 1] + off + 2560));
      } else B[s & 1][4] = cat8(trd(bx[sl >> 1] + so), trd(bx[sl >> 1] + so + 2560));
    };
    auto issue_q = [&](auto kc) AI {
      constexpr int k = decltype(kc)::v;
      dma<ka_q(k), k * 64 * 3456>(rQ, vq, wl1);
    };
    auto blk = [&](auto bc, int i) AI { piece(bc, i); if (i == 2) piece(bc, 3); };
    auto warm = [&](int h, bool up) AI {
      const int li = min((h * 256 + gw) * 4 + min(lane, 3), 1943), row = li / 9;
      touch<KA_DEAD>(up ? rU : rG, (unsigned)(row * 9216 + (li - row * 9) * 128 + xcd * 1152), lds0 + w * 256);
    };
    auto slot = [&](auto gc) AI {
      constexpr int g = decltype(gc)::v;
      if constexpr (g < 3) { blk(IC<3>{}, g); }
      if constexpr (g >= 3 && g < 6) blk(IC<4>{}, g - 3);
      if constexpr (g >= 6 && g < 9) blk(IC<5>{}, g - 6);
      if constexpr (g >= 15 && g < 18) blk(IC<6>{}, g - 15);
      if constexpr (g >= 18 && g < 21) blk(IC<7>{}, g - 18);
      if constexpr (g == 22) warm(0, false);
      if constexpr (g == 25) warm(0, true);
      if constexpr (g >= 27 && g < 30) blk(IC<8>{}, g - 27);
      if constexpr (g >= 30 && g < 35) { if (w < 5) issue_q(IC<g - 30>{}); }
      if constexpr (g == 35) warm(1, false);
      if constexpr (g == 38) warm(1, true);
      if constexpr (g >= 39 && g < 43) { if (w < 5) issue_q(IC<g - 34>{}); }
    };
    auto step = [&](auto sc) AI {
      constexpr int s = decltype(sc)::v;
      constexpr bool nx = s < 17;
      acc[0] = MFMA16(B[s & 1][0], a[s], acc[0]); acc[1] = MFMA16(B[s & 1][1], a[s], acc[1]); SBAR();
      if constexpr (nx && (s & 1)) readA((s + 1) >> 1);
      if constexpr (nx) { readB(s + 1, 0); readB(s + 1, 1); }
      slot(IC<3 * s>{}); SBAR();
      acc[2] = MFMA16(B[s & 1][2], a[s], acc[2]); acc[3] = MFMA16(B[s & 1][3], a[s], acc[3]); SBAR();
      if constexpr (nx) { readB(s + 1, 2); readB(s + 1, 3); }
      slot(IC<3 * s + 1>{}); SBAR();
      acc[4] = MFMA16(B[s & 1][4], a[s], acc[4]);
      gram = MFMA16(a[s], a[s], gram); sx = MFMA16(ones, a[s], sx);
      SBAR();
      if constexpr (nx) readB(s + 1, 4);
      slot(IC<3 * s + 2>{}); SBAR();
      if (s >= 13 || (w & 1) == (s & 1)) {
#pragma unroll
        for (int i = 0; i < 5; ++i) cs[i] = MFMA16(B[s & 1][i], ones, cs[i]);
      }
      SBAR();
    };
    STAMP(1);
    wait_bar<9>();
    STAMP(2);
    readA(0);
#pragma unroll
    for (int i = 0; i < 5; ++i) readB(0, i);
    f32x4 csp[5];
    static_for<0, 9>([&](auto ic) AI {
      constexpr int KB = decltype(ic)::v;
      step(IC<2 * KB>{});
      if constexpr (KB == 2) {
        if (tid < 36) {
          float s = 0.f;
#pragma unroll
          for (int i = 0; i < 9; ++i) {
            const u32x2 v = *(LAS u32x2*)(L + KA_WO + tid * 80 + ot + i * 8);
            s += (bflo(v[0]) + bfhi(v[0])) + (bflo(v[1]) + bfhi(v[1]));
          }
          *(LAS float*)(L + KA_RSP + tid * 4) = s;
        }
      }
      if constexpr (KB == 6) {
        if (fr == 0) {
#pragma unroll
          for (int i = 0; i < 5; ++i) *(LAS f32x4*)(L + KA_CS + w * 320 + (i * 4 + fq) * 16) = cs[i];
        }
      }
      if constexpr (KB == 0) wait_bar<4>();
      if constexpr (KB == 2) wait_bar<3>();
      if constexpr (KB == 4) wait_bar<5>();
      if constexpr (KB == 6) { if (w < 5) wait_bar<7>(); else wait_bar<2>(); }
      if constexpr (KB == 6) {
#pragma unroll
        for (int i = 0; i < 5; ++i) csp[i] = *(LAS f32x4*)(L + KA_CS + (w ^ 1) * 320 + (i * 4 + fq) * 16);
      }
      step(IC<2 * KB + 1>{});
    });
    STAMP(3);
    float meanT, rstdT;
    {
      const int rr = fr & 3;
      float g = rr == 0 ? gram[0] : rr == 1 ? gram[1] : rr == 2 ? gram[2] : gram[3];
      g = sum_fq(q4 == fq ? g : 0.f);
      meanT = sx[0] * (1.f / 576.f);
      rstdT = __builtin_amdgcn_rsqf(fmaxf(g * (1.f / 576.f) - meanT * meanT, 0.f) + 1e-5f);
      const lptr exw = L + KA_EX + (w * 16 + fr) * 160 + fq * 8;
#pragma unroll
      for (int i = 0; i < 5; ++i) {
        float v[4];
#pragma unroll
        for (int k = 0; k < 4; ++k) {
          const float t = (acc[i][k] - meanT * (cs[i][k] + csp[i][k])) * rstdT;
          if (i < 2) v[k] = phi_f(t);
          else if (i < 4) v[k] = t;
          else { const float ph = phi_f(t); v[k] = fq == 0 ? ph : (fq == 1 ? t : 1.0f); }
        }
        *(LAS u32x2*)(exw + i * 32) = (u32x2){pk2(v[0], v[1]), pk2(v[2], v[3])};
      }
    }
    STAMP(4);
    asm volatile("s_waitcnt vmcnt(0) lgkmcnt(0)\n\ts_barrier" : "+v"(ep), "+v"(tab)::"memory");
    const unsigned recb = WS_REC + p * 16 * REC;
    {
      const int sa = (0x44101100 >> (4 * w)) & 7, sb = (0x43443232 >> (4 * w)) & 7;
      const lptr ex = L + KA_EX + (4 * fq + q4) * 160 + pp * 8;
      const lptr exa = ex + sa * 32, exb = ex + sb * 32;
      bf16x8 fa[4], fb[4];
#pragma unroll
      for (int k = 0; k < 4; ++k) {
        fa[k] = cat8(trd(exa + k * 5120), trd(exa + k * 5120 + 2560));
        fb[k] = cat8(trd(exb + k * 5120), trd(exb + k * 5120 + 2560));
      }
      const f32x4 rsp = *(LAS f32x4*)(L + KA_RSP + min(lane, 8) * 16);
      f32x4 T = z4;
#pragma unroll
      for (int k = 0; k < 4; ++k) T = MFMA16(fa[k], fb[k], T);
      if (tab[0] >= 197 * 16) T = rsp;
      st16_dev(rW, recb + tt * REC + tab[0], __builtin_bit_cast(u32x4, T));
    }
    STAMP(5);
    tag0 = __builtin_amdgcn_readfirstlane((ep[0] * 0x9E3779B1u + 0x7F4A7C15u + ep[1]) & ~15u);
    tag1 = __builtin_amdgcn_readfirstlane((ep[1] ^ (ep[0] >> 7)) * 0x85EBCA6Bu + 0x165667B1u);
    if (tag1 == (unsigned)__builtin_amdgcn_readfirstlane(ep[1])) tag1 ^= 0x5bd1e995u;
    f32x4 qa[3];
    {
#pragma unroll
      for (int i = 0; i < 3; ++i) qa[i] = z4;
      lptr bq[2], bqp[2];
      bq[0] = L + (2 * (4 * (fq & 1) + q4) + (fq >> 1)) * 80 + o8 + 64;
      bqp[0] = bq[0] - 64 + 8 * pp;
      bq[1] = bq[0] + 4 * KA_SLOT; bqp[1] = bqp[0] + 4 * KA_SLOT;
      PIN(bq[1]); PIN(bqp[1]);
      bf16x8 b[3][3];
      auto rq = [&](int s) AI {
        const int h = s < 10 ? 1 : 0, o = (s < 10 ? s : s - 10) * 2560;
        b[s % 3][0] = cat8(trd(bqp[h] + o), trd(bqp[h] + o + 1280));
        b[s % 3][1] = cat8(trd(bqp[h] + o + 32), trd(bqp[h] + o + 1280 + 32));
        b[s % 3][2] = cat8(trd(bq[h] + o), trd(bq[h] + o + 1280));
      };
      auto wo_dma = [&]() AI {
        const int j = w & 3;
        {
          const unsigned vo0 = mko(j, false), vo1 = mko(j + 4, false), vo8 = mko(8, false) + j * 144 * 1152, wj = lds0 + j * 1024;
          static_for<0, 8>([&](auto bc) AI {
            constexpr int b = decltype(bc)::v;
            dma<KB_WO + b * 9216, b * 72 * 1152>(rO, vo0, wj);
            dma<KB_WO + b * 9216 + 4096, b * 72 * 1152>(rO, vo1, wj);
          });
          dma<KB_WO + 8192, 0>(rO, vo8, lds0 + j * 18432);
          dma<KB_WO + 9216 + 8192, 72 * 1152>(rO, vo8, lds0 + j * 18432);
          dma<KB_WO + 8 * 9216, 0>(rO, mko(j, true), wj);
          dma<KB_WO + 8 * 9216 + 4096, 0>(rO, mko(j + 4, true), wj);
          if (j == 0) dma<KB_WO + 8 * 9216 + 8192, 0>(rO, mko(8, true), lds0);
        }
      };
      if (w >= 4) {
        f32x4 qc[3] = {z4, z4, z4};
        const int j = w & 3;
#pragma unroll
        for (int i = 0; i < 3; ++i) {
          const int kb = j + 4 * i, o = kb < 5 ? 4 * KA_SLOT + kb * 5120 : (kb - 5) * 5120;
          if (i < 2 || j == 0) {
#pragma unroll
            for (int h = 0; h < 2; ++h) {
              const lptr q0 = bqp[0] + o + h * 2560, q1 = bq[0] + o + h * 2560;
              qc[0] = MFMA16(cat8(trd(q0), trd(q0 + 1280)), ones, qc[0]);
              qc[1] = MFMA16(cat8(trd(q0 + 32), trd(q0 + 1280 + 32)), ones, qc[1]);
              qc[2] = MFMA16(cat8(trd(q1), trd(q1 + 1280)), ones, qc[2]);
            }
          }
        }
        if (fr == 0) {
#pragma unroll
          for (int i = 0; i < 3; ++i) *(LAS f32x4*)(L + KA_QC + ((i * 4 + fq) * 4 + j) * 16) = qc[i];
        }
        asm volatile("s_waitcnt vmcnt(0) lgkmcnt(0)\n\ts_barrier" ::: "memory");
        wo_dma();
      }
      rq(0); rq(1);
      static_for<0, 18>([&](auto ic) AI {
        constexpr int s = decltype(ic)::v;
        if constexpr (s < 16) rq(s + 2);
#pragma unroll
        for (int i = 0; i < 3; ++i) qa[i] = MFMA16(b[s % 3][i], a[s], qa[i]);
        SBAR();
        if constexpr (s == PUBSTEP) {
          if (w < 4) {
            asm volatile("s_waitcnt vmcnt(0)\n\ts_barrier" ::: "memory");
            if (tid == 0) st8_dev(rW, WS_F1 + (p * 16 + tt) * 64, (u32x2){tag0, tag1});
            STAMP(6);
          }
        }
      });
      STAMP(7);
      u32x4 v[16];
      if (w < 4) {
        poll(rW, WS_F1 + (p * 16 + fr) * 64, tag0, tag1, 0);
        STAMP(8);
        const unsigned vo = recb + min(tid, 205) * 16;
#pragma unroll
        for (int t = 0; t < 16; ++t) asm volatile("buffer_load_dwordx4 %0, %1, %2, %3 offen sc1" : "=v"(v[t]) : "v"(vo), "s"(rW), "s"(t * REC) : "memory");
      }
      u32x4 pqf0;
      u32x2 pqf1;
      {
        float pq[3][4];
#pragma unroll
        for (int i = 0; i < 3; ++i) {
          const lptr qp = L + KA_QC + (i * 4 + fq) * 64;
          const f32x4 cq = (*(LAS f32x4*)qp + *(LAS f32x4*)(qp + 16)) + (*(LAS f32x4*)(qp + 32) + *(LAS f32x4*)(qp + 48));
#pragma unroll
          for (int k = 0; k < 4; ++k) pq[i][k] = phi_f((qa[i][k] - meanT * cq[k]) * rstdT);
        }
        pqf0 = (u32x4){pk2(pq[0][0], pq[0][1]), pk2(pq[0][2], pq[0][3]), pk2(pq[1][0], pq[1][1]), pk2(pq[1][2], pq[1][3])};
        pqf1 = (u32x2){fq == 0 ? pk2(pq[2][0], pq[2][1]) : 0u, fq == 0 ? pk2(pq[2][2], pq[2][3]) : 0u};
        PIN(pqf0); PIN(pqf1);
      }
      STAMP(9);
      if (w < 4) {
        asm volatile("s_waitcnt vmcnt(0)"
                     : "+v"(v[0]), "+v"(v[1]), "+v"(v[2]), "+v"(v[3]), "+v"(v[4]), "+v"(v[5]), "+v"(v[6]), "+v"(v[7]), "+v"(v[8]), "+v"(v[9]),
                       "+v"(v[10]), "+v"(v[11]), "+v"(v[12]), "+v"(v[13]), "+v"(v[14]), "+v"(v[15])::"memory");
#define BC(i) __builtin_bit_cast(f32x4, v[i])
        const f32x4 s = (((BC(0) + BC(1)) + (BC(2) + BC(3))) + ((BC(4) + BC(5)) + (BC(6) + BC(7)))) +
                        (((BC(8) + BC(9)) + (BC(10) + BC(11))) + ((BC(12) + BC(13)) + (BC(14) + BC(15))));
#undef BC
        const unsigned cm = tab[1] >> 24;
        const lptr cd = L + (tab[1] & 0xffffff);
        if (cm < 32) {
          *(LAS u32x2*)cd = (u32x2){pk2((cm & 1) ? s[0] : 0.f, (cm & 2) ? s[1] : 0.f), pk2((cm & 4) ? s[2] : 0.f, (cm & 8) ? s[3] : 0.f)};
          if (cm >= 16)
            *(LAS u32x2*)(cd + 96) = (u32x2){pk2((cm & 1) ? 0.f : s[0], (cm & 2) ? 0.f : s[1]), pk2((cm & 4) ? 0.f : s[2], (cm & 8) ? 0.f : s[3])};
        } else if (cm == 32) {
          *(LAS f32x4*)cd = s * (1.f / 576.f);
        }
      }
      lds_bar();
      STAMP(10);
      {
        const lptr kr = L + KA_KVI + fr * 96 + fq * 8;
        const bf16x8 b0 = __builtin_bit_cast(bf16x8, pqf0), b1 = __builtin_bit_cast(bf16x8, ((u32x4){pqf1[0], pqf1[1], 0u, 0u}));
        f32x4 T[3], rm[3];
#pragma unroll
        for (int i = 0; i < 3; ++i) {
          const u32x2 a0 = *(LAS u32x2*)(kr + i * 1536), a1 = *(LAS u32x2*)(kr + i * 1536 + 32), a2 = *(LAS u32x2*)(kr + i * 1536 + 64);
          rm[i] = *(LAS f32x4*)(L + KA_RS + (i < 2 ? 16 * i + 4 * fq : 32) * 4);
          T[i] = MFMA16(__builtin_bit_cast(bf16x8, ((u32x4){a0[0], a0[1], a1[0], a1[1]})), b0, z4);
          T[i] = MFMA16(__builtin_bit_cast(bf16x8, ((u32x4){a2[0], a2[1], 0u, 0u})), b1, T[i]);
        }
        const float d0 = sum_fq(fq == 1 ? T[2][0] : 0.f), d1 = sum_fq(fq == 1 ? T[2][1] : 0.f);
        const float i0 = __builtin_amdgcn_rcpf(d0 + 1e-6f), i1 = __builtin_amdgcn_rcpf(d1 + 1e-6f), il = fq == 0 ? i0 : i1;
        const u32x4 om = {pk2(T[0][0] * i0, T[0][1] * i0), pk2(T[0][2] * i0, T[0][3] * i0), pk2(T[1][0] * il, T[1][1] * il), pk2(T[1][2] * i1, T[1][3] * i1)};
        const u32x2 ol = {pk2(T[2][0] * i1, T[2][1] * i1), pk2(T[2][2] * i1, T[2][3] * i1)};
        const unsigned afb = WS_AF + (tok0 >> 6) * 18 * 4096 + ((tok0 >> 4) & 3) * 1024;
        st16_dev(rW, afb + p * 4096 + lane * 16, om);
        if (fq == 0) st8_dev(rW, afb + (16 + (p >> 3)) * 4096 + (fr + 16 * (p & 3)) * 16 + ((p >> 2) & 1) * 8, ol);
        float pm = ((bflo(om[0]) * rm[0][0] + bfhi(om[0]) * rm[0][1]) + (bflo(om[1]) * rm[0][2] + bfhi(om[1]) * rm[0][3])) +
                   ((bflo(om[2]) * rm[1][0] + bfhi(om[2]) * rm[1][1]) + (bflo(om[3]) * rm[1][2] + bfhi(om[3]) * rm[1][3]));
        const float pl = (bflo(ol[0]) * rm[2][0] + bfhi(ol[0]) * rm[2][1]) + (bflo(ol[1]) * rm[2][2] + bfhi(ol[1]) * rm[2][3]);
        pm = sum_fq(pm + (fq == 0 ? pl : 0.f)) + (p == 0 ? meanT : 0.f);
        if (fq == 0) st4_dev(rW, WS_PM + p * 8192 + (tok0 + fr) * 4, __float_as_uint(pm));
      }
    }
    STAMP(11);
    asm volatile("s_waitcnt vmcnt(0) lgkmcnt(0)\n\ts_barrier" ::: "memory");
    if (tid == 0) st8_dev(rW, WS_F2 + (p * 16 + tt) * 64, (u32x2){tag0, tag1});
    STAMP(12);
  }

  {
    const int t64 = (xcd & 3) * 8 + (r & 7), j = (xcd >> 2) * 4 + (r >> 3);
    const int msub = w & 3, kh = w >> 2;
    const int tok = t64 * 64 + msub * 16 + fr;
    u32x2 xv[3];
    {
      const char* xr = (const char*)x + (size_t)tok * 1152 + 144 * j;
      xv[0] = *(const u32x2*)(xr + kh * 64 + 8 * fq);
      xv[1] = *(const u32x2*)(xr + kh * 64 + 32 + 8 * fq);
      xv[2] = *(const u32x2*)(xr + 128 + 8 * (fq & 1));
    }
    bf16x8 bv[9][5];
    {
      lptr pb[2], p4[2];
      pb[0] = L + KB_WO + (2 * (4 * (fq & 1) + q4) + (fq >> 1)) * 144 + kh * 4608 + 8 * pp;
      p4[0] = pb[0] - 8 * pp + 128 + 8 * (pp & 1);
      pb[1] = pb[0] + 5 * 9216; p4[1] = p4[0] + 5 * 9216;
      PIN(pb[1]); PIN(p4[1]);
#pragma unroll
      for (int kb = 0; kb < 9; ++kb) {
        const int h = kb >= 5, o = (kb - 5 * h) * 9216;
#pragma unroll
        for (int i = 0; i < 4; ++i) bv[kb][i] = cat8(trd(pb[h] + o + 32 * i), trd(pb[h] + o + 32 * i + 2304));
        bv[kb][4] = cat8(trd(p4[h] + o), trd(p4[h] + o + 2304));
      }
#pragma unroll
      for (int kb = 0; kb < 9; ++kb)
#pragma unroll
        for (int i = 0; i < 5; ++i) PIN(bv[kb][i]);
    }
    poll(rW, WS_F2 + (fr * 16 + (t64 >> 1)) * 64, tag0, tag1, 0);
    if (blockIdx.x == 0 && tid == 0) st8_dev(rW, WS_EPOCH, (u32x2){tag0, tag1});
    STAMP(13);
    u32x4 av[9];
    {
      const unsigned va = WS_AF + t64 * 18 * 4096 + kh * 4096 + msub * 1024 + lane * 16;
#pragma unroll
      for (int kb = 0; kb < 9; ++kb) av[kb] = ld16_dev(rW, va, kb * 8192);
    }
    float pmv[4];
#pragma unroll
    for (int i = 0; i < 4; ++i) pmv[i] = __uint_as_float(ld4_dev(rW, WS_PM + (4 * fq + i) * 8192 + tok * 4));
    SBAR();
    f32x4 acc[5];
#pragma unroll
    for (int i = 0; i < 5; ++i) acc[i] = z4;
#pragma unroll
    for (int kb = 0; kb < 9; ++kb)
#pragma unroll
      for (int i = 0; i < 5; ++i) acc[i] = MFMA16(bv[kb][i], __builtin_bit_cast(bf16x8, av[kb]), acc[i]);
    const float mean2 = sum_fq((pmv[0] + pmv[1]) + (pmv[2] + pmv[3]));
    STAMP(14);
    STAMP(15);
    {
      const lptr xs = L + 106496 + (msub * 2 + kh) * 3072 + lane * 16;
      *(LAS f32x4*)(xs) = kh == 0 ? acc[2] : acc[0];
      *(LAS f32x4*)(xs + 1024) = kh == 0 ? acc[3] : acc[1];
      *(LAS f32x4*)(xs + 2048) = acc[4];
    }
    lds_bar();
    f32x4 o[3];
    {
      const lptr xs = L + 106496 + (msub * 2 + (1 - kh)) * 3072 + lane * 16;
      o[0] = (kh == 0 ? acc[0] : acc[2]) + *(LAS f32x4*)(xs);
      o[1] = (kh == 0 ? acc[1] : acc[3]) + *(LAS f32x4*)(xs + 1024);
      o[2] = acc[4] + *(LAS f32x4*)(xs + 2048);
    }
    u32x2 pk[3];
    float ssq = 0.f;
#pragma unroll
    for (int i = 0; i < 3; ++i) {
      pk[i] = (u32x2){pk2(bflo(xv[i][0]) + o[i][0] - mean2, bfhi(xv[i][0]) + o[i][1] - mean2),
                      pk2(bflo(xv[i][1]) + o[i][2] - mean2, bfhi(xv[i][1]) + o[i][3] - mean2)};
      const float a0 = bflo(pk[i][0]), a1 = bfhi(pk[i][0]), a2 = bflo(pk[i][1]), a3 = bfhi(pk[i][1]);
      const float q = (a0 * a0 + a1 * a1) + (a2 * a2 + a3 * a3);
      ssq += (i < 2 || (kh == 0 && fq < 2)) ? q : 0.f;
    }
    ssq = sum_fq(ssq);
    const unsigned hb = WS_HCF + ((tok >> 7) * 18 * 8 + ((tok >> 4) & 7)) * 1024;
    st16_dev(rW, hb + (2 * j + kh) * 8192 + lane * 16, (u32x4){pk[0][0], pk[0][1], pk[1][0], pk[1][1]});
    if (kh == 0 && fq < 2) st8_dev(rW, hb + (16 + (j >> 2)) * 8192 + (fr + 16 * (j & 3)) * 16 + fq * 8, pk[2]);
    if (fq == 0) st4_dev(rW, WS_SSQ + (2 * j + kh) * 8192 + tok * 4, __float_as_uint(ssq));
    if (j == 0 && kh == 0 && fq == 1) st4_dev(rW, WS_MEAN2 + tok * 4, __float_as_uint(mean2));
    STAMP(16);
    asm volatile("s_waitcnt vmcnt(0) lgkmcnt(0)\n\ts_barrier" ::: "memory");
    if (tid == 0) st8_dev(rW, WS_F3 + (t64 * 8 + j) * 64, (u32x2){tag0, tag1});
    STAMP(17);
  }

  const int fs = xcd * 2 + (r & 1), mh = r >> 1;
  const int hh = fs & 1, t64o = mh * 2 + hh;
  {
    const rsrc_t rD = mkrs(wd, 576 * 4608 * 2);
    const int wm = w & 1, wn = w >> 1;
    const bool lo = w < 4;
    unsigned vb0, vb1, vbx;
    auto mkb = [&](int pi, bool left) AI {
      const int c = pi * 64 + lane, rho = c / 36, cs = c - rho * 36;
      const int row = left ? 72 * ((rho >> 2) & 3) + 64 + 4 * (rho >> 4) + (rho & 3) : rho;
      return (unsigned)(row * 9216 + ((((cs >> 1) ^ ((rho >> 2) & 1)) << 1) + (cs & 1)) * 16 + fs * 576);
    };
    vb0 = mkb(w, false); vb1 = mkb(w + 8, false); vbx = mkb(16 + (w & 1), false);
    const rsrc_t rXt = w < 2 ? rG : rU;
    const unsigned wlx = lds0 + (w < 2 ? F_G : F_U) + (16 + (w & 1)) * 1024;
    const unsigned vA = WS_HCF + mh * 18 * 8192 + tid * 16;
    const unsigned vd = (unsigned)((fs * 288 + w * 8 + (lane >> 3)) * 1152 + (((((lane & 7) >> 1) ^ (fq & 3)) << 1) + (lane & 1)) * 16);
    const unsigned vd2 = vd + (hh ? -288 * 1152 : 288 * 1152);
    auto p1 = [&](auto sc, int i) AI {
      constexpr int s = decltype(sc)::v, so = (s % 3) * F_STG;
      constexpr int go = (s < 16 ? 72 * (s >> 1) + 32 * (s & 1) : 288 * (s - 16)) * 9216;
      if (i == 0) dma<so + F_G, go>(rG, vb0, wl1);
      if (i == 1) dma<so + F_U, go>(rU, vb0, wl1);
      if (i == 2) dma_dev<so, s * 8192>(rW, vA, wl1);
      if (i == 3) dma<so + F_G + 8192, go>(rG, vb1, wl1);
      if (i == 4) dma<so + F_U + 8192, go>(rU, vb1, wl1);
      if (i == 5) dma<so, go>(rXt, vbx, wlx);
    };
    auto p2 = [&](auto cc, auto ic) AI {
      constexpr int c = decltype(cc)::v, i = decltype(ic)::v;
      dma<f_wslot(f_reg(c)) + i * 8192, (c % 9) * 128 + i * 64 * 1152>(rD, c < 9 ? vd : vd2, wl1);
    };
    auto slot = [&](auto stc, auto slc) AI {
      constexpr int st = decltype(stc)::v, sl = decltype(slc)::v;
      if constexpr (st < 18) {
        constexpr int pl = sl == 0 ? 0 : sl == 2 ? 1 : sl == 3 ? 2 : sl == 5 ? 3 : sl == 6 ? 4 : sl == 8 ? 5 : -1;
        constexpr int ph = sl == 1 ? 0 : sl == 2 ? 1 : sl == 4 ? 2 : sl == 5 ? 3 : sl == 7 ? 4 : -1;
        if constexpr (pl >= 0 && pl == ph) p1(stc, pl);
        else if constexpr (pl >= 0) { if (lo) p1(stc, pl); }
        else if constexpr (ph >= 0) { if (!lo) p1(stc, ph); }
      } else if constexpr (st < 21) {
        if constexpr ((sl & 1) == 0) { if (lo) p2(IC<st - 18>{}, IC<(sl >> 1)>{}); }
        else { if (!lo) p2(IC<st - 18>{}, IC<(sl >> 1)>{}); }
      }
    };
    auto wts = [&]() AI {
      static_for<0, 3>([&](auto sc) AI {
        p1(sc, 0); p1(sc, 1); p1(sc, 3); p1(sc, 4);
        if (lo) p1(sc, 5);
      });
    };
    if (w == (mh & 7)) { poll(rW, WS_F3 + (mh * 16 + fr) * 64, tag0, tag1, 0); asm volatile("s_barrier" ::: "memory"); wts(); }
    else { wts(); asm volatile("s_barrier" ::: "memory"); }
    STAMP(18);
    dma_dev<F_SSQ, 0>(rW, (unsigned)(WS_SSQ + (2 * w + (lane >> 5)) * 8192 + mh * 512 + (lane & 31) * 16), wl1);
    p1(IC<0>{}, 2); p1(IC<1>{}, 2); p1(IC<2>{}, 2);
    const rsrc_t rT = mkrs((w & 1) ? wu : wg, 576 * 4608 * 2);
    const unsigned wlt = lds0 + w * 256;
    unsigned vt;
    {
      const int g = min(gw >> 1, 125), row = g / 3;
      vt = (unsigned)(row * 9216 + ((g - row * 3) * 3 + min(lane, 2)) * 128 + xcd * 1152);
    }
    const int gb = fq & 1;
    const int rowb = (4 * fq + q4) * 576;
    lptr pE[3], pO[3], pX[3], pA[2];
    pE[0] = L + F_G + rowb + 128 * wn + 32 * gb + 8 * pp;
    pO[0] = L + F_G + rowb + 128 * wn - 32 * gb + 8 * pp;
    pX[0] = L + rowb + (pp < 2 ? F_G : F_U) + ((16 + (wn >> 1)) ^ gb) * 32 + 16 * (wn & 1) + 8 * (pp & 1);
    pA[0] = L + (wm * 4) * 1024 + lane * 16;
    pE[1] = pE[0] + F_STG; pO[1] = pO[0] + F_STG; pE[2] = pE[0] + 2 * F_STG; pO[2] = pO[0] + 2 * F_STG;
    pX[1] = pX[0]; pX[2] = pX[0] + 2 * F_STG; pA[1] = pA[0] + 2 * F_STG;
    PIN(pE[1]); PIN(pO[1]); PIN(pE[2]); PIN(pO[2]); PIN(pX[2]); PIN(pA[1]);

    f32x4 acc[9][4];
    bf16x8 A[2][4], B[9];
    auto rdA = [&](int s, int m) AI { A[s & 1][m] = ldfrag((s % 3 == 2 ? pA[1] : pA[0] + (s % 3) * F_STG) + m * 1024); };
    auto rdB = [&](int s, int n) AI {
      const int b = s % 3;
      if (n < 8) {
        const int nn = n >> 1, off = ((n & 1) ? F_U - F_G : 0) + 32 * nn;
        const lptr q = (nn & 1) ? pO[b] : pE[b];
        B[n] = cat8(trd(q + off), trd(q + off + 16 * 576));
      } else {
        const lptr q = pX[b] + (b == 1 ? F_STG : 0);
        B[n] = cat8(trd(q), trd(q + 16 * 576));
      }
    };
    auto grh = [&](int s, int n, int h) AI {
#pragma unroll
      for (int m = 2 * h; m < 2 * h + 2; ++m) {
        if (s == 0) MFMA_NEW(acc[n][m], B[n], A[s & 1][m]); else MFMA_ACC(acc[n][m], B[n], A[s & 1][m]);
      }
      SBAR();
    };
    wait_bar<2>();
    STAMP(19);
#pragma unroll
    for (int m = 0; m < 4; ++m) rdA(0, m);
#pragma unroll
    for (int n = 0; n < 8; ++n) rdB(0, n);
    if (tid < 128) {
      float s = 0.f;
#pragma unroll
      for (int i = 0; i < 16; ++i) s += *(LAS float*)(L + F_SSQ + i * 512 + tid * 4);
      *(LAS float*)(L + F_RS + tid * 4) = __builtin_amdgcn_rsqf(s * (1.f / 576.f) + 1e-5f);
    }
    SBAR();
    if (w >= 4) __builtin_amdgcn_s_setprio(1);
    float rs[4];
    static_for<0, 18>([&](auto ic) AI {
      constexpr int s = decltype(ic)::v;
      constexpr bool nx = s < 17;
      using SP = IC<(s >= 1 ? s + 2 : 99)>;
      using SN = IC<s + 3>;
      grh(s, 0, 0); slot(SP{}, IC<6>{}); rdB(s, 8); SBAR(); grh(s, 0, 1);
      grh(s, 1, 0); slot(SP{}, IC<7>{}); SBAR(); grh(s, 1, 1);
      grh(s, 2, 0); slot(SP{}, IC<8>{}); SBAR(); grh(s, 2, 1);
      if (s == 0) wait_bar<1>(); else if (s <= 15) wait_bar<6>(); else if (s == 16) wait_bar<5>(); else lds_bar();
      if constexpr (s <= 8) { touch_o<F_RS + 1024, (216 + 42 * s) * 9216>(rT, vt, wlt); SBAR(); }
      if constexpr (s == 9) vt = (unsigned)(xcd * 5184 + gw * 3 + min(lane, 2)) * 128;
      if constexpr (s >= 9 && s <= 15) { touch_o<F_RS + 1024, (s - 9) * 768 * 128>(rD, vt, wlt); SBAR(); }
      if constexpr (s == 13) { vb0 = mkb(w, true); vb1 = mkb(w + 8, true); vbx = mkb(16 + (w & 1), true); SBAR(); }
      grh(s, 3, 0); slot(SN{}, IC<0>{}); if (nx) { rdA(s + 1, 0); rdA(s + 1, 1); rdB(s + 1, 0); } SBAR(); grh(s, 3, 1);
      grh(s, 4, 0); slot(SN{}, IC<1>{}); if (nx) { rdA(s + 1, 2); rdA(s + 1, 3); rdB(s + 1, 1); } SBAR(); grh(s, 4, 1);
      grh(s, 5, 0); slot(SN{}, IC<2>{}); if (nx) { rdB(s + 1, 2); rdB(s + 1, 3); } SBAR(); grh(s, 5, 1);
      grh(s, 6, 0); slot(SN{}, IC<3>{}); if (nx) { rdB(s + 1, 4); rdB(s + 1, 5); } SBAR(); grh(s, 6, 1);
      grh(s, 7, 0); slot(SN{}, IC<4>{}); if (nx) { rdB(s + 1, 6); }
      if (s == 16) {
#pragma unroll
        for (int m = 0; m < 4; ++m) rs[m] = *(LAS float*)(L + F_RS + (wm * 64 + m * 16 + fr) * 4);
      }
      SBAR(); grh(s, 7, 1);
      grh(s, 8, 0); slot(SN{}, IC<5>{}); SBAR(); grh(s, 8, 1); if (nx) { rdB(s + 1, 7); } SBAR();
    });
    asm volatile("s_nop 7\n\ts_nop 1" ::: "memory");
    SBAR();
    STAMP(20);
    slot(IC<20>{}, IC<6>{}); slot(IC<20>{}, IC<7>{}); slot(IC<20>{}, IC<8>{});
    {
      const bool own = wm == hh;
      const lptr hb = L + F_H;
      const unsigned hx = WS_HX + (mh * 16 + fs) * 36864;
      auto glu = [&](float g, float u, float rsv, float rnv) AI { return __builtin_amdgcn_rcpf(1.f + __builtin_amdgcn_exp2f(g * rnv)) * (u * rsv); };
#pragma unroll
      for (int pr = 0; pr < 2; ++pr) {
#pragma unroll
        for (int m = 0; m < 4; ++m) {
          const float rsv = rs[m], rnv = rsv * -1.4426950408889634f;
          const f32x4 g0 = acc[4 * pr][m], u0 = acc[4 * pr + 1][m], g1 = acc[4 * pr + 2][m], u1 = acc[4 * pr + 3][m];
          u32x4 o;
          o[0] = pk2(glu(g0[0], u0[0], rsv, rnv), glu(g0[1], u0[1], rsv, rnv));
          o[1] = pk2(glu(g0[2], u0[2], rsv, rnv), glu(g0[3], u0[3], rsv, rnv));
          o[2] = pk2(glu(g1[0], u1[0], rsv, rnv), glu(g1[1], u1[1], rsv, rnv));
          o[3] = pk2(glu(g1[2], u1[2], rsv, rnv), glu(g1[3], u1[3], rsv, rnv));
          const int off = (2 * wn + pr) * 4096 + m * 1024 + lane * 16;
          if (own) *(LAS u32x4*)(hb + off) = o; else st16_dev(rW, hx + off, o);
        }
      }
      const bool up = lane >= 32;
#pragma unroll
      for (int mp = 0; mp < 2; ++mp) {
        float g[4], u[4];
#pragma unroll
        for (int rr = 0; rr < 4; ++rr) {
          auto sw = __builtin_amdgcn_permlane32_swap(__float_as_uint(acc[8][2 * mp][rr]), __float_as_uint(acc[8][2 * mp + 1][rr]), false, false);
          g[rr] = __uint_as_float(sw[0]);
          u[rr] = __uint_as_float(sw[1]);
        }
        const float rsv = up ? rs[2 * mp + 1] : rs[2 * mp], rnv = rsv * -1.4426950408889634f;
        const int m = 2 * mp + (up ? 1 : 0);
        const int gam = 2 * wn + gb;
        const int off = 8 * 4096 + m * 1024 + (fr + 16 * (gam & 3)) * 16 + (gam >> 2) * 8;
        const u32x2 o = {pk2(glu(g[0], u[0], rsv, rnv), glu(g[1], u[1], rsv, rnv)), pk2(glu(g[2], u[2], rsv, rnv), glu(g[3], u[3], rsv, rnv))};
        if (own) *(LAS u32x2*)(hb + off) = o; else st8_dev(rW, hx + off, o);
      }
    }
    STAMP(21);
    wait_bar<8>();
    lptr pD[4];
    {
      const int rho = 4 * fq + q4;
      pD[0] = L + rho * 128 + ((wn ^ ((rho >> 1) & 3)) * 32) + 8 * pp;
      pD[1] = pD[0] + f_wslot(1); pD[2] = pD[0] + f_wslot(2); pD[3] = pD[0] + f_wslot(3);
      PIN(pD[1]); PIN(pD[2]); PIN(pD[3]);
    }
    auto rdD = [&](int c, int k) AI { B[k] = cat8(trd(pD[f_reg(c)] + k * 4096), trd(pD[f_reg(c)] + k * 4096 + 2048)); };
    bf16x8 HA[9][2];
    lptr hr = L + F_H + wm * 2048 + lane * 16;
    auto rdH = [&](int k) AI { HA[k][0] = ldfrag(hr + k * 4096); HA[k][1] = ldfrag(hr + k * 4096 + 1024); };
#pragma unroll
    for (int k = 0; k < 9; ++k) { if (k < 8) rdD(0, k); rdH(k); }
    hr -= F_H;
    const unsigned part = WS_PART + (((fs >> 1) * 32 + t64o) * 72 + w) * 1024 + lane * 16;
    const unsigned f4 = WS_F4 + (t64o * 8 + (fs >> 1)) * 64;
    const unsigned vhx = WS_HX + (mh * 16 + (fs ^ 1)) * 36864 + tid * 16;
    f32x4 o[9][2];
    auto st1 = [&](int cb) AI {
      const f32x4 a = o[cb][0], b = o[cb][1];
      __builtin_amdgcn_raw_buffer_store_b128((u32x4){pk2(a[0], a[1]), pk2(a[2], a[3]), pk2(b[0], b[1]), pk2(b[2], b[3])}, rW, part, cb * 8192, 16);
    };
    auto gd = [&](int c, int k) AI {
#pragma unroll
      for (int m = 0; m < 2; ++m) o[c % 9][m] = MFMA16(B[k], HA[k][m], (c < 9 && k == 0) ? z4 : o[c % 9][m]);
      SBAR();
    };
    SBAR();
    const unsigned f5p = WS_F5 + (mh * 16 + (fs ^ 1)) * 64;
    u32x2 pf;
    auto pcs = [&](auto stc, auto nc) AI {
      constexpr int st = decltype(stc)::v;
      static_for<0, decltype(nc)::v>([&](auto ic) AI {
        constexpr int i = decltype(ic)::v;
        if constexpr (st < 18) p2(stc, ic); else dma_dev<i * 8192, i * 8192>(rW, vhx, wl1);
      });
    };
    auto slot2 = [&](auto ivc, auto slc) AI {
      constexpr int iv = decltype(ivc)::v, sl = decltype(slc)::v;
      constexpr int s1 = iv == 0 ? 3 : iv <= 3 ? iv + 4 : iv == 4 ? 18 : iv <= 9 ? iv + 3 : iv + 4;
      if constexpr (iv >= 0 && iv <= 13) {
        if constexpr (sl == LOSL) { if (lo) { pcs(IC<s1>{}, IC<5>{}); if constexpr (iv == 0 || iv == 9) pcs(IC<s1 + 1>{}, IC<5>{}); } }
        if constexpr (sl == HISL) { if (!lo) { pcs(IC<s1>{}, IC<4>{}); if constexpr (iv == 0 || iv == 9) pcs(IC<s1 + 1>{}, IC<4>{}); } }
      }
    };
    static_for<0, 18>([&](auto ic) AI {
      constexpr int c = decltype(ic)::v;
      constexpr bool nx = c < 17;
      using P = IC<c - 1>;
      gd(c, 0); slot2(P{}, IC<6>{}); rdD(c, 8); if constexpr (c == 8) rdH(0); if constexpr (c == 9) rdH(8); SBAR();
      gd(c, 1); slot2(P{}, IC<7>{}); if constexpr (c == 8) rdH(1); SBAR();
      gd(c, 2); slot2(P{}, IC<8>{}); if constexpr (c == 8) rdH(2); if constexpr (c >= 10) st1(c - 10); SBAR();
      constexpr int N = c == 0 ? 4 : c == 3 ? 9 : c <= 6 ? 8 : c <= 9 ? 4 : c == 10 ? 9 : c <= 14 ? 10 : c == 15 ? 6 : 2;
      if constexpr (c == 4) asm volatile("s_waitcnt vmcnt(8) lgkmcnt(0)\n\ts_barrier" : "+v"(pf)::"memory");
      else wait_bar<N>();
      if constexpr (c == 0) STAMP(26);
      if constexpr (c == 3) STAMP(27);
      if constexpr (c == 4) STAMP(28);
      if constexpr (c == 8) STAMP(29);
      if constexpr (c == 12) STAMP(30);
      if constexpr (c == 16) STAMP(31);
      if constexpr (c == 1) { if (tid == 0) st8_dev(rW, WS_F5 + (mh * 16 + fs) * 64, (u32x2){tag0, tag1}); }
      if constexpr (c >= 12) { if (tid == 0) st8_dev(rW, f4, (u32x2){tag0 + (c - 11), tag1}); }
      gd(c, 3);
      if constexpr (c == 2) asm volatile("buffer_load_dwordx2 %0, %1, %2, 0 offen sc1" : "=v"(pf) : "v"(f5p), "s"(rW) : "memory");
      if constexpr (c == 4) { if (!__all(pf[0] == tag0 && pf[1] == tag1)) poll(rW, f5p, tag0, tag1, 0); }
      slot2(ic, IC<0>{}); if (nx) { rdD(c + 1, 0); rdD(c + 1, 1); } if constexpr (c == 8) rdH(3); SBAR();
      gd(c, 4); slot2(ic, IC<1>{}); if (nx) { rdD(c + 1, 2); rdD(c + 1, 3); } if constexpr (c == 8) rdH(4); SBAR();
      gd(c, 5); slot2(ic, IC<2>{}); if (nx) rdD(c + 1, 4); if constexpr (c == 8) rdH(5); SBAR();
      gd(c, 6); slot2(ic, IC<3>{}); if (nx) rdD(c + 1, 5); if constexpr (c == 8) rdH(6); SBAR();
      gd(c, 7); slot2(ic, IC<4>{}); if (nx) rdD(c + 1, 6); if constexpr (c == 8) rdH(7); SBAR();
      gd(c, 8); slot2(ic, IC<5>{}); if (nx) rdD(c + 1, 7); SBAR();
    });
    st1(8);
    STAMP(22);
    asm volatile("s_waitcnt vmcnt(0)\n\ts_barrier" ::: "memory");
    if (tid == 0) st8_dev(rW, f4, (u32x2){tag0 + 9, tag1});
    STAMP(23);
  }

  if (w < 6) {
    const int q = fs >> 1, wm = q & 1, wn = q >> 1, ms0 = hh * 4 + wm * 2;
    const int c0 = w < 3 ? 2 * w : w + 3;
    auto hoff = [&](int c) AI {
      const int cg = 4 * (c * 4 + wn) + fq, j = cg / 18, gl = cg - 18 * j;
      return (unsigned)(WS_HCF + ((mh * 18 + (gl < 16 ? 2 * j + (gl >> 3) : 16 + (j >> 2))) * 8 + ms0) * 1024 +
                        (fr + 16 * (gl < 16 ? (gl & 3) : (j & 3))) * 16 + (gl < 16 ? ((gl >> 2) & 1) : gl - 16) * 8);
    };
    const unsigned ho0 = hoff(c0), ho1 = hoff(c0 + 1);
    u32x2 hc[2][2];
    float mu[2];
#pragma unroll
    for (int i = 0; i < 2; ++i) {
      hc[0][i] = ld8_dev(rW, ho0 + i * 1024);
      hc[1][i] = ld8_dev(rW, ho1 + i * 1024);
      mu[i] = __uint_as_float(ld4_dev(rW, WS_MEAN2 + (mh * 128 + (ms0 + i) * 16 + fr) * 4));
    }
    poll(rW, WS_F4 + (t64o * 8 + (lane & 7)) * 64, tag0, tag1, w < 3 ? c0 + 2 : 9);
    STAMP(24);
    const unsigned vo = WS_PART + (t64o * 72 + q) * 1024 + c0 * 8192 + lane * 16;
    auto fin = [&](int b, const u32x4(&v)[8]) AI {
      float s[8];
#pragma unroll
      for (int k = 0; k < 8; ++k) s[k] = 0.f;
#pragma unroll
      for (int f = 0; f < 8; ++f)
#pragma unroll
        for (int k = 0; k < 4; ++k) { s[2 * k] += bflo(v[f][k]); s[2 * k + 1] += bfhi(v[f][k]); }
#pragma unroll
      for (int i = 0; i < 2; ++i) {
        const u32x2 o = {pk2(s[4 * i] + (bflo(hc[b][i][0]) + mu[i]), s[4 * i + 1] + (bfhi(hc[b][i][0]) + mu[i])),
                         pk2(s[4 * i + 2] + (bflo(hc[b][i][1]) + mu[i]), s[4 * i + 3] + (bfhi(hc[b][i][1]) + mu[i]))};
        *(u32x2*)(out + (size_t)(mh * 128 + (ms0 + i) * 16 + fr) * 576 + ((c0 + b) * 4 + wn) * 16 + 4 * fq) = o;
      }
    };
    u32x4 v0[8], v1[8];
#pragma unroll
    for (int f = 0; f < 8; ++f) v0[f] = ld16_dev(rW, vo, f * 2304 * 1024);
    if (w < 3) {
#pragma unroll
      for (int f = 0; f < 8; ++f) v1[f] = ld16_dev(rW, vo + 8192, f * 2304 * 1024);
    }
    fin(0, v0);
    if (w < 3) fin(1, v1);
    STAMP(25);
  }
  asm volatile(".Lmk_end:");
}

extern "C" void kernel_launch(void* const* d_in, const int* in_sizes, int n_in, void* d_out, int out_size, void* d_ws, size_t ws_size,
                              hipStream_t stream) {
  mk_block<<<256, 512, 0, stream>>>((const u16*)d_in[0], (const u16*)d_in[1], (const u16*)d_in[2], (const u16*)d_in[4],
                                    (const u16*)d_in[5], (const u16*)d_in[6], (char*)d_ws, (u16*)d_out);
}
```
